```python
import torch

def _softmax(x, axis=-1):
    return torch.softmax(x, dim=axis)
def _gelu(x):
    return torch.nn.functional.gelu(x, approximate="tanh")
def _silu(x):
    return torch.nn.functional.silu(x)
def _sigmoid(x):
    return torch.sigmoid(x)
def _layernorm(x, axis=-1, eps=1e-5):
    m = x.mean(dim=axis, keepdim=True)
    v = x.var(dim=axis, keepdim=True, unbiased=False)
    return (x - m) / torch.sqrt(v + eps)
def _scatter_add(tgt, idx, src, axis=0):
    return tgt.clone().index_add_(axis, idx, src)
def _segment_sum(src, seg, n):
    out = torch.zeros((n,) + src.shape[1:], dtype=src.dtype)
    return out.index_add_(0, seg, src)
def _pad(x, pw):
    flat = tuple(v for pair in reversed(pw) for v in pair)
    return torch.nn.functional.pad(x, flat)

kernel_name = 'gated_mix__ssm_conv_h3__grouped_kv_mqa__prompt'

def setup_inputs():

    _w = (lambda a: (lambda t: t.to(torch.bfloat16) if t.is_floating_point() else t)(torch.as_tensor(a)))
    def _A_setup():
        B, S, D = 1, 2048, 512
        ks = 4
        return {
            "q": _w((lambda *s: torch.randn(*s, dtype=torch.bfloat16) * 0.05)(B, S, D)),
            "k": _w((lambda *s: torch.randn(*s, dtype=torch.bfloat16) * 0.2)(B, S, D)),
            "v": _w((lambda *s: torch.randn(*s, dtype=torch.bfloat16) * 0.2)(B, S, D)),
            "shift_k": _w((lambda *s: torch.randn(*s, dtype=torch.bfloat16) * 0.2)(D, ks) * 0.5),
            "ssm_k": _w((lambda *s: torch.randn(*s, dtype=torch.bfloat16) * 0.8)(D, S) * (1.0 / S ** 0.5)),
            "ssm_D": _w((lambda *s: torch.randn(*s, dtype=torch.bfloat16) * 0.2)(D)),
        }
    _A_in = _A_setup()

    def _B_setup():
        B, S, D, H = 1, 2048, 512, 32
        Dh = D // H
        return {
            "x": _w((lambda *s: torch.randn(*s, dtype=torch.bfloat16) * 0.05)(B, S, D)),
            "wq": _w((lambda *s: torch.randn(*s, dtype=torch.bfloat16) * 0.8)(D, H * Dh)),
            "wk": _w((lambda *s: torch.randn(*s, dtype=torch.bfloat16) * 0.8)(D, Dh)),
            "wv": _w((lambda *s: torch.randn(*s, dtype=torch.bfloat16) * 0.05)(D, Dh)),
            "wo": _w((lambda *s: torch.randn(*s, dtype=torch.bfloat16) * 0.05)(H * Dh, D)),
        }
    _B_in = _B_setup()

    return {
        "A_q": _A_in["q"],
        "A_k": _A_in["k"],
        "A_v": _A_in["v"],
        "A_shift_k": _A_in["shift_k"],
        "A_ssm_k": _A_in["ssm_k"],
        "A_ssm_D": _A_in["ssm_D"],
        "B_x": _B_in["x"],
        "B_wq": _B_in["wq"],
        "B_wk": _B_in["wk"],
        "B_wv": _B_in["wv"],
        "B_wo": _B_in["wo"],
        "gate_w": _w((lambda *s: torch.randn(*s, dtype=torch.bfloat16) * 0.05)(512) * 16.0),
    }

def reference(A_q, A_k, A_v, A_shift_k, A_ssm_k, A_ssm_D, B_x, B_wq, B_wk, B_wv, B_wo, gate_w):
    def _A_ref(q, k, v, shift_k, ssm_k, ssm_D):
        B, S, D = q.shape
        ks = shift_k.shape[-1]
        k_pad = _pad(k, ((0, 0), (ks - 1, 0), (0, 0)))
        k_shift = torch.zeros_like(k)
        for t in range(ks):
            k_shift = k_shift + k_pad[:, t:t + S, :] * shift_k[:, ks - 1 - t][None, None, :]
        kv = k_shift * v
        L = 2 * S
        kv_pad = _pad(kv, ((0, 0), (0, S), (0, 0)))
        kf_pad = _pad(ssm_k, ((0, 0), (0, S)))
        kv_f = (lambda x, axes=(-2,-1): torch.fft.rfft2(x.float(), dim=axes))(kv_pad[:, None, :, :], axes=(1, 2))
        kf_f = (lambda x, axes=(-2,-1): torch.fft.rfft2(x.float(), dim=axes))(kf_pad[:, None, :], axes=(1, 2))
        kf_f = (lambda x, *d: x.permute(*d))(kf_f, 1, 2, 0)
        y_f = kv_f * kf_f[None, :, :, :]
        y = (lambda x, s, axes=(-2,-1): torch.fft.irfft2(x, s=s, dim=axes).to(torch.bfloat16))(y_f, (1, L), axes=(1, 2))[:, 0, :S, :]
        y = y + kv * ssm_D[None, None, :]
        return q * y

    def _B_ref(x, wq, wk, wv, wo):
        B, S, D = x.shape
        H = 32
        Dh = D // H
        q = (lambda x, *d: x.permute(*d))((x @ wq).reshape(B, S, H, Dh), 0, 2, 1, 3)
        k = x @ wk
        v = x @ wv
        scores = torch.einsum("bhqd,bkd->bhqk", q, k) * (Dh ** -0.5)
        p = _softmax(scores, axis=-1)
        o = torch.einsum("bhqk,bkd->bhqd", p, v)
        return (lambda x, *d: x.permute(*d))(o, 0, 2, 1, 3).reshape(B, S, H * Dh) @ wo

    x = A_q
    g = _sigmoid((x * gate_w).sum(-1, keepdims=True))
    a = _A_ref(x, A_k, A_v, A_shift_k, A_ssm_k, A_ssm_D)
    b = _B_ref(x, B_wq, B_wk, B_wv, B_wo)
    return g * a + (1 - g) * b
```

```cpp
#include <hip/hip_runtime.h>
#include <hip/hip_bf16.h>
#include <stdint.h>

namespace {

typedef unsigned short u16;
typedef __attribute__((ext_vector_type(8))) __bf16 bf16x8;
typedef __attribute__((ext_vector_type(16))) __bf16 bf16x16;
typedef __attribute__((ext_vector_type(2))) __bf16 bf16x2;
typedef __attribute__((ext_vector_type(4))) short s16x4;
typedef __attribute__((ext_vector_type(2))) float f32x2;
typedef __attribute__((ext_vector_type(4))) float f32x4;
typedef __attribute__((ext_vector_type(8))) float f32x8;
typedef __attribute__((ext_vector_type(16))) float f32x16;
typedef __attribute__((ext_vector_type(2))) unsigned u32x2;
typedef __attribute__((ext_vector_type(4))) unsigned u32x4;
typedef __amdgpu_buffer_rsrc_t rsrc_t;
#define LDSP __attribute__((address_space(3)))

constexpr int S_LEN = 2048, DM = 512;

constexpr unsigned WS_Q  = 0;
constexpr unsigned WS_UR = 2u << 20;
constexpr unsigned WS_O  = 4u << 20;
constexpr unsigned WS_YT = 6u << 20;
constexpr unsigned WS_KV = 8u << 20;
constexpr unsigned KF_BYTES = 65536, VF_BYTES = 64 * 1088, KV_BYTES = KF_BYTES + VF_BYTES;
constexpr unsigned WS_KN = WS_KV + KV_BYTES;
constexpr unsigned WS_G  = WS_KN + 1024;

constexpr float QSCALE = 0.25f * 1.4426950408889634f;

__device__ __forceinline__ rsrc_t mk_rsrc(const void* p, unsigned bytes) {
    return __builtin_amdgcn_make_buffer_rsrc((void*)p, (short)0, (int)bytes, 0x00020000);
}
__device__ __forceinline__ void dma16(rsrc_t rs, unsigned voff, unsigned soff, unsigned ldsaddr) {
    asm volatile("s_nop 4\n\ts_mov_b32 m0, %0\n\ts_nop 0\n\tbuffer_load_dwordx4 %1, %2, %3 offen lds"
                 :: "s"(ldsaddr), "v"(voff), "s"(rs), "s"(soff) : "memory");
}
__device__ __forceinline__ void bload16(u32x4& dst, rsrc_t rs, unsigned voff) {
    asm volatile("buffer_load_dwordx4 %0, %1, %2, 0 offen" : "=v"(dst) : "v"(voff), "s"(rs) : "memory");
}
__device__ __forceinline__ void bload8(u32x2& dst, rsrc_t rs, unsigned voff) {
    asm volatile("buffer_load_dwordx2 %0, %1, %2, 0 offen" : "=v"(dst) : "v"(voff), "s"(rs) : "memory");
}
__device__ __forceinline__ void bload4(unsigned& dst, rsrc_t rs, unsigned voff) {
    asm volatile("buffer_load_dword %0, %1, %2, 0 offen" : "=v"(dst) : "v"(voff), "s"(rs) : "memory");
}
template <int N> __device__ __forceinline__ void wait_vm() { asm volatile("s_waitcnt vmcnt(%0)" :: "i"(N) : "memory"); }
__device__ __forceinline__ void wait_lgkm0() { asm volatile("s_waitcnt lgkmcnt(0)" ::: "memory"); }
__device__ __forceinline__ void barrier() { asm volatile("" ::: "memory"); __builtin_amdgcn_s_barrier(); asm volatile("" ::: "memory"); }

__device__ __forceinline__ unsigned pack2(float a, float b) {
    f32x2 v = {a, b};
    return __builtin_bit_cast(unsigned, __builtin_convertvector(v, bf16x2));
}
__device__ __forceinline__ float u2f(unsigned u) { return __builtin_bit_cast(float, u); }
__device__ __forceinline__ float bflo(unsigned u) { return __builtin_bit_cast(float, u << 16); }
__device__ __forceinline__ float bfhi(unsigned u) { return __builtin_bit_cast(float, u & 0xffff0000u); }
__device__ __forceinline__ float rbf(float a) { return bflo(pack2(a, 0.f)); }

__device__ __forceinline__ u32x4 lds16(const char LDSP* p) { return *(const u32x4 LDSP*)p; }
__device__ __forceinline__ u32x2 ldstr(const char LDSP* p) {
    return __builtin_bit_cast(u32x2, __builtin_amdgcn_ds_read_tr16_b64_v4i16((s16x4 LDSP*)p));
}
__device__ __forceinline__ bf16x8 asbf(u32x4 v) { return __builtin_bit_cast(bf16x8, v); }
__device__ __forceinline__ u32x4 cat2(u32x2 a, u32x2 b) { return u32x4{a[0], a[1], b[0], b[1]}; }
__device__ __forceinline__ f32x16 mfma32(u32x4 a, u32x4 b, f32x16 c) { return __builtin_amdgcn_mfma_f32_32x32x16_bf16(asbf(a), asbf(b), c, 0, 0, 0); }
__device__ __forceinline__ f32x4 mfma16(u32x4 a, u32x4 b, f32x4 c) { return __builtin_amdgcn_mfma_f32_16x16x32_bf16(asbf(a), asbf(b), c, 0, 0, 0); }

constexpr unsigned G_XS = 0, G_WS = 65536;

__device__ __forceinline__ void gemm_issue(rsrc_t rsX, rsrc_t rsW, unsigned lds0, int w, int lane, int tt, int j) {
    const unsigned r = lane >> 3, c = lane & 7;
    const unsigned xrow = 8 * w + r;
    const unsigned vx = (tt * 64 + xrow) * 1024 + ((c ^ ((xrow >> 1) & 7)) << 4);
    const unsigned vw = xrow * 1024 + j * 128 + ((c ^ (((r >> 1) & 1) << 2)) << 4);
#pragma unroll
    for (int kc = 0; kc < 8; ++kc) {
        dma16(rsX, vx, kc * 128, lds0 + G_XS + kc * 8192 + w * 1024);
        dma16(rsW, vw, kc * 65536, lds0 + G_WS + kc * 8192 + w * 1024);
    }
}

struct GemmAddr { unsigned xa[4]; unsigned wa; };
__device__ __forceinline__ GemmAddr gemm_addr(int wm, int wn, int lane) {
    GemmAddr g;
    const unsigned row = 32 * wm + (lane & 31), h = lane >> 5;
#pragma unroll
    for (int s = 0; s < 4; ++s) g.xa[s] = G_XS + row * 128 + (((2 * s + h) ^ ((row >> 1) & 7)) << 4);
    const unsigned G = lane >> 4, q = (lane & 15) >> 2, p = lane & 3;
    const unsigned c = 4 * wn + 2 * (G & 1) + (p >> 1);
    g.wa = G_WS + (8 * (G >> 1) + q) * 128 + ((c ^ (((q >> 1) & 1) << 2)) << 4) + 8 * (p & 1);
    return g;
}
__device__ __forceinline__ void gemm_chunk(const char LDSP* sm, const GemmAddr& g, int kc, f32x16& acc) {
#pragma unroll
    for (int s = 0; s < 4; ++s) {
        const u32x4 xb = lds16(sm + g.xa[s] + kc * 8192);
        const u32x2 w0 = ldstr(sm + g.wa + kc * 8192 + s * 2048);
        const u32x2 w1 = ldstr(sm + g.wa + kc * 8192 + s * 2048 + 512);
        acc = mfma32(cat2(w0, w1), xb, acc);
    }
}
__device__ __forceinline__ void quads_to_rows(u32x2 (&p)[4], u32x4& r0, u32x4& r1) {
    auto s0 = __builtin_amdgcn_permlane32_swap(p[0][0], p[1][0], false, false);
    auto s1 = __builtin_amdgcn_permlane32_swap(p[0][1], p[1][1], false, false);
    auto s2 = __builtin_amdgcn_permlane32_swap(p[2][0], p[3][0], false, false);
    auto s3 = __builtin_amdgcn_permlane32_swap(p[2][1], p[3][1], false, false);
    r0 = u32x4{s0[0], s1[0], s0[1], s1[1]};
    r1 = u32x4{s2[0], s3[0], s2[1], s3[1]};
}

constexpr unsigned K1_WE = 131072;
constexpr unsigned K1_KT = K1_WE + 16384;
constexpr unsigned K1_LDS = K1_KT + 67 * 128;

__global__ __launch_bounds__(512) void k1_proj(const u16* __restrict__ x, const u16* __restrict__ kin, const u16* __restrict__ vin,
                                               const u16* __restrict__ shiftk, const u16* __restrict__ wq, const u16* __restrict__ wk,
                                               const u16* __restrict__ wv, const u16* __restrict__ gw, char* __restrict__ ws) {
    __shared__ __attribute__((aligned(16))) char smem[K1_LDS];
    const char LDSP* sm = (const char LDSP*)smem;
    char LDSP* smw = (char LDSP*)smem;
    const unsigned lds0 = (unsigned)(size_t)sm;
    const int lane = threadIdx.x & 63;
    const int w = __builtin_amdgcn_readfirstlane(threadIdx.x >> 6);
    const int bid = blockIdx.x;
    const int tt = (bid & 7) * 4 + (bid >> 6), j = (bid >> 3) & 7;

    const rsrc_t rsX = mk_rsrc(x, S_LEN * DM * 2), rsW = mk_rsrc(wq, DM * DM * 2);
    const rsrc_t rsK = mk_rsrc(kin, S_LEN * DM * 2), rsV = mk_rsrc(vin, S_LEN * DM * 2), rsS = mk_rsrc(shiftk, DM * 4 * 2);

    const int oct = lane & 7, tl = 8 * w + (lane >> 3);
    u32x4 vv, sk0, sk1, sk2, sk3;
    bload16(vv, rsV, (tt * 64 + tl) * 1024 + j * 128 + oct * 16);
    {
        const unsigned so = (j * 64 + oct * 8) * 8;
        bload16(sk0, rsS, so); bload16(sk1, rsS, so + 16); bload16(sk2, rsS, so + 32); bload16(sk3, rsS, so + 48);
    }
    {
        const int row = 8 * w + (lane >> 3);
        dma16(rsK, (unsigned)((tt * 64 - 3 + row) * 1024 + j * 128 + (lane & 7) * 16), 0, lds0 + K1_KT + w * 1024);
        if (w == 0) {
            if (lane < 24) dma16(rsK, (unsigned)((tt * 64 + 61 + (lane >> 3)) * 1024 + j * 128 + (lane & 7) * 16), 0, lds0 + K1_KT + 8 * 1024);
        }
    }
    if (j < 2) {
        const rsrc_t rsE = mk_rsrc(j == 0 ? wk : wv, DM * 16 * 2);
#pragma unroll
        for (int i = 0; i < 2; ++i) {
            const unsigned prow = (w * 2 + i) * 32 + (lane >> 1);
            const unsigned grow = prow ^ (((prow >> 3) & 1) << 2);
            dma16(rsE, grow * 32 + (lane & 1) * 16, 0, lds0 + K1_WE + (w * 2 + i) * 1024);
        }
    } else if (j == 2) {
        if (w == 0) dma16(mk_rsrc(gw, DM * 2), lane * 16, 0, lds0 + K1_WE);
    }
    gemm_issue(rsX, rsW, lds0, w, lane, tt, j);

    asm volatile("s_waitcnt vmcnt(16)" : "+v"(vv), "+v"(sk0), "+v"(sk1), "+v"(sk2), "+v"(sk3) :: "memory");
    barrier();
    unsigned kvp[4];
    {
        u32x4 kr[4];
#pragma unroll
        for (int i = 0; i < 4; ++i) kr[i] = lds16(sm + K1_KT + (tl + i) * 128 + oct * 16);
        const u32x4 sk[4] = {sk0, sk1, sk2, sk3};
        float r[8];
#pragma unroll
        for (int c = 0; c < 8; ++c) {
            const unsigned s01 = sk[c >> 1][(c & 1) * 2], s23 = sk[c >> 1][(c & 1) * 2 + 1];
            auto kk = [&](int i) { return (c & 1) ? bfhi(kr[i][c >> 1]) : bflo(kr[i][c >> 1]); };
            float a = kk(0) * bfhi(s23);
            a = fmaf(kk(1), bflo(s23), a);
            a = fmaf(kk(2), bfhi(s01), a);
            a = fmaf(kk(3), bflo(s01), a);
            const float vf = (c & 1) ? bfhi(vv[c >> 1]) : bflo(vv[c >> 1]);
            r[c] = rbf(a) * vf;
        }
#pragma unroll
        for (int c = 0; c < 4; ++c) kvp[c] = pack2(r[2 * c], r[2 * c + 1]);
    }

    const int wm = w & 1, wn = (w >> 1) & 1;
    const GemmAddr ga = gemm_addr(wm, wn, lane);
    f32x16 acc = {};
    f32x4 eacc = {};
    unsigned ex[2], ee[2];
    {
        const unsigned row = 16 * (w & 3) + (lane & 15), g = lane >> 4;
        ex[0] = G_XS + row * 128 + ((g ^ ((row >> 1) & 7)) << 4);
        ex[1] = G_XS + row * 128 + (((4 + g) ^ ((row >> 1) & 7)) << 4);
        const unsigned q = (lane & 15) >> 2, p = lane & 3;
        ee[0] = K1_WE + (8 * g + q + 4 * (0 ^ (g & 1))) * 32 + 8 * p;
        ee[1] = K1_WE + (8 * g + q + 4 * (1 ^ (g & 1))) * 32 + 8 * p;
        if (j == 2) ee[0] = K1_WE + g * 16;
    }
#pragma unroll
    for (int kc = 0; kc < 8; ++kc) {
        switch (kc) {
            case 0: wait_vm<14>(); break; case 1: wait_vm<12>(); break; case 2: wait_vm<10>(); break; case 3: wait_vm<8>(); break;
            case 4: wait_vm<6>(); break;  case 5: wait_vm<4>(); break;  case 6: wait_vm<2>(); break;  default: wait_vm<0>(); break;
        }
        if (kc <= 1) wait_lgkm0();
        barrier();
        if (kc == 0) {
            const unsigned mp = 63 - tl;
#pragma unroll
            for (int c = 0; c < 8; ++c) {
                const unsigned ch = 8 * oct + c;
                const u16 val = (u16)((c & 1) ? (kvp[c >> 1] >> 16) : kvp[c >> 1]);
                *(u16 LDSP*)(smw + K1_KT + ch * 128 + (((mp >> 3) ^ oct) << 4) + (mp & 7) * 2) = val;
            }
        }
        if (kc == 1) {
            const unsigned ch = 8 * w + (lane >> 3), c = lane & 7;
            const u32x4 t = lds16(sm + K1_KT + ch * 128 + ((c ^ (ch >> 3)) << 4));
            *(u32x4*)(ws + WS_UR + (size_t)(j * 64 + ch) * 4096 + (1984 - 64 * tt + 8 * c) * 2) = t;
        }
        if (w < 4) {
            gemm_chunk(sm, ga, kc, acc);
            if (j < 3) {
#pragma unroll
                for (int s = 0; s < 2; ++s) {
                    const u32x4 xb = lds16(sm + ex[s] + kc * 8192);
                    u32x4 eb;
                    if (j == 2) eb = lds16(sm + ee[0] + kc * 128 + s * 64);
                    else eb = cat2(ldstr(sm + ee[0] + kc * 2048 + s * 1024), ldstr(sm + ee[1] + kc * 2048 + s * 1024));
                    if (j == 0) eacc = mfma16(eb, xb, eacc);
                    else eacc = mfma16(xb, eb, eacc);
                }
            }
        }
    }

    if (w < 4) {
        u32x2 p[4];
#pragma unroll
        for (int q = 0; q < 4; ++q) {
            p[q][0] = pack2(rbf(acc[4 * q]) * QSCALE, rbf(acc[4 * q + 1]) * QSCALE);
            p[q][1] = pack2(rbf(acc[4 * q + 2]) * QSCALE, rbf(acc[4 * q + 3]) * QSCALE);
        }
        u32x4 r0, r1;
        quads_to_rows(p, r0, r1);
        char* qp = ws + WS_Q + (size_t)(tt * 64 + 32 * wm + (lane & 31)) * 1024 + (j * 64 + 32 * wn + 8 * (lane >> 5)) * 2;
        *(u32x4*)qp = r0;
        *(u32x4*)(qp + 32) = r1;

        const int g = lane >> 4, n = lane & 15;
        if (j == 0) {
            const unsigned p0 = pack2(eacc[0], eacc[1]), p1 = pack2(eacc[2], eacc[3]);
            const int key = tt * 64 + 16 * w + n;
            *(u32x2*)(ws + WS_KV + (key >> 5) * 1024 + (g >> 1) * 512 + (key & 31) * 16 + (g & 1) * 8) = u32x2{p0, p1};
            float ss = bflo(p0) * bflo(p0) + bfhi(p0) * bfhi(p0) + bflo(p1) * bflo(p1) + bfhi(p1) * bfhi(p1);
            ss += __shfl_xor(ss, 16); ss += __shfl_xor(ss, 32);
#pragma unroll
            for (int o = 1; o < 16; o <<= 1) ss = fmaxf(ss, __shfl_xor(ss, o));
            if (lane == 0) ((float*)(ws + WS_KN))[tt * 4 + w] = ss;
        } else if (j == 1) {
            const unsigned p0 = pack2(eacc[0], eacc[1]), p1 = pack2(eacc[2], eacc[3]);
            const int tile = 2 * tt + (w >> 1), pp = w & 1;
            char* vb = ws + WS_KV + KF_BYTES + tile * 1088 + pp * 544;
            *(u32x2*)(vb + ((g >> 1) * 16 + n) * 16 + (g & 1) * 8) = u32x2{p0, p1};
            if (lane < 2) {
                const unsigned f = lane == 0 ? 0x3f803f80u : 0u;
                *(u32x4*)(vb + (32 + lane) * 16) = u32x4{f, f, f, f};
            }
            float m = fmaxf(fmaxf(fabsf(bflo(p0)), fabsf(bfhi(p0))), fmaxf(fabsf(bflo(p1)), fabsf(bfhi(p1))));
            if (!(m == m) || (p0 & 0x7f807f80u) == 0x7f807f80u) m = __builtin_inff();
#pragma unroll
            for (int o = 1; o < 64; o <<= 1) m = fmaxf(m, __shfl_xor(m, o));
            if (lane == 0) ((float*)(ws + WS_KN))[128 + tt * 4 + w] = m;
        } else if (j == 2) {
            if (n == 0) {
                f32x4 gv;
#pragma unroll
                for (int r = 0; r < 4; ++r) gv[r] = rbf(1.f / (1.f + __expf(-rbf(eacc[r]))));
                *(f32x4*)(ws + WS_G + (tt * 64 + 16 * w + 4 * g) * 4) = gv;
            }
        }
    }
}

constexpr unsigned K2_K = 0, K2_V = 65536, K2_H = KV_BYTES, K2_HC = 4160, K2_U = K2_H + 4 * K2_HC, K2_UC = 4608;
constexpr unsigned K2_LDS = K2_U + 2 * K2_UC;
constexpr unsigned K2_PART = K2_H;
constexpr int SPLIT = 28;

#define AE(X, i) "v_exp_f32 v[" #X "+" #i "], v[" #X "+" #i "]\n\t"
#define ATILE(X, Y, t)                                                                       \
    AE(X, 0) AE(X, 1) AE(X, 2) AE(X, 3)                                                      \
    "s_waitcnt lgkmcnt(2)\n\t"                                                               \
    "v_mfma_f32_32x32x16_bf16 v[" #Y ":" #Y "+15], %1, %4, 0\n\t"                            \
    "ds_read_b128 %1, %6 offset:(" #t "+2)*1024\n\t"                                         \
    AE(X, 4) AE(X, 5) AE(X, 6) AE(X, 7) AE(X, 8) AE(X, 9)                                    \
    "s_waitcnt lgkmcnt(2)\n\t"                                                               \
    "v_smfmac_f32_32x32x32_bf16 %0, %2, v[" #X ":" #X "+7], %5\n\t"                          \
    "ds_read_b128 %2, %7 offset:(" #t "+1)*1088\n\t"                                         \
    AE(X, 10) AE(X, 11) AE(X, 12) AE(X, 13) AE(X, 14) AE(X, 15)                              \
    "s_waitcnt lgkmcnt(2)\n\t"                                                               \
    "s_nop 0\n\t"                                                                            \
    "v_smfmac_f32_32x32x32_bf16 %0, %3, v[" #X "+8:" #X "+15], %5\n\t"                       \
    "ds_read_b128 %3, %7 offset:(" #t "+1)*1088+544\n\t"
#define ATILE2(t0, t1) ATILE(0, 16, t0) ATILE(16, 0, t1)

__device__ __forceinline__ void attn_fast(f32x16& O, u32x4 q, unsigned kaddr, unsigned vaddr, int longrun, int dobar) {
    u32x4 kf, v0, v1;
    const unsigned idx = 0xddddu;
    asm volatile(
        "ds_read_b128 %1, %6\n\t"
        "s_waitcnt lgkmcnt(0)\n\t"
        "v_mfma_f32_32x32x16_bf16 v[0:15], %1, %4, 0\n\t"
        "ds_read_b128 %1, %6 offset:1024\n\t"
        "ds_read_b128 %2, %7\n\t"
        "ds_read_b128 %3, %7 offset:544\n\t"
        "s_nop 10\n\t"
        ATILE2(0, 1)
        "s_cmp_eq_u32 %9, 0\n\t"
        "s_cbranch_scc1 1f\n\t"
        "s_waitcnt vmcnt(0)\n\t"
        "s_barrier\n\t"
        "1:\n\t"
        ATILE2(2, 3) ATILE2(4, 5) ATILE2(6, 7) ATILE2(8, 9) ATILE2(10, 11) ATILE2(12, 13) ATILE2(14, 15)
        ATILE2(16, 17) ATILE2(18, 19) ATILE2(20, 21) ATILE2(22, 23) ATILE2(24, 25) ATILE2(26, 27)
        "s_cmp_eq_u32 %8, 0\n\t"
        "s_cbranch_scc1 2f\n\t"
        ATILE2(28, 29) ATILE2(30, 31) ATILE2(32, 33) ATILE2(34, 35)
        "2:\n\t"
        "s_waitcnt lgkmcnt(0)\n\t"
        "s_nop 15\n\t"
        "s_nop 7\n\t"
        : "+v"(O), "=&v"(kf), "=&v"(v0), "=&v"(v1)
        : "v"(q), "v"(idx), "v"(kaddr), "v"(vaddr), "s"(longrun), "s"(dobar)
        : "v0", "v1", "v2", "v3", "v4", "v5", "v6", "v7", "v8", "v9", "v10", "v11", "v12", "v13", "v14", "v15",
          "v16", "v17", "v18", "v19", "v20", "v21", "v22", "v23", "v24", "v25", "v26", "v27", "v28", "v29", "v30", "v31",
          "scc", "memory");
}

__device__ __forceinline__ void attn_slow(f32x16& O, float& m, u32x4 q, const char LDSP* sm, unsigned kaddr, unsigned vaddr, int ntiles, int dobar) {
    if (dobar) { wait_vm<0>(); barrier(); }
    for (int t = 0; t < ntiles; ++t) {
        const u32x4 kf = lds16(sm + kaddr + t * 1024);
        const u32x4 v0 = lds16(sm + vaddr + t * 1088), v1 = lds16(sm + vaddr + t * 1088 + 544);
        f32x16 s = mfma32(kf, q, f32x16{});
        float tm = s[0];
#pragma unroll
        for (int r = 1; r < 16; ++r) tm = fmaxf(tm, s[r]);
        tm = fmaxf(tm, __shfl_xor(tm, 32));
        const float mn = fmaxf(m, tm);
        const float alpha = __builtin_amdgcn_exp2f(m - mn);
        m = mn;
#pragma unroll
        for (int r = 0; r < 16; ++r) { O[r] *= alpha; s[r] = __builtin_amdgcn_exp2f(s[r] - mn); }
        const f32x8 slo = __builtin_shufflevector(s, s, 0, 1, 2, 3, 4, 5, 6, 7), shi = __builtin_shufflevector(s, s, 8, 9, 10, 11, 12, 13, 14, 15);
        O = __builtin_amdgcn_smfmac_f32_32x32x32_bf16(asbf(v0), __builtin_bit_cast(bf16x16, slo), O, 0xdddd, 0, 0);
        O = __builtin_amdgcn_smfmac_f32_32x32x32_bf16(asbf(v1), __builtin_bit_cast(bf16x16, shi), O, 0xdddd, 0, 0);
    }
}

template <int SET>
__device__ __forceinline__ void conv_wave(const char LDSP* sm, int ch, int lane, f32x4 (&acc)[4]) {
    constexpr int T0 = SET ? 1 : 0, T1 = SET ? 2 : 3, T2 = SET ? 5 : 4, T3 = SET ? 6 : 7;
    constexpr int NP = T3 + 1;
    const int b = lane & 15, g = lane >> 4;
    const int c = (b + 1) & 1;
    const unsigned ha = K2_H + (ch * 2 + c) * K2_HC + ((b + 1 - c) / 2 + 4 * g) * 4;
    const unsigned ua = K2_U + ch * K2_UC + (2032 - 16 * b + 8 * g) * 2 - 7 * 512;
#pragma unroll
    for (int i = 0; i < 4; ++i) acc[i] = f32x4{};
#pragma unroll 1
    for (int r = 0; r < 8; ++r) {
        u32x4 A[NP], B[NP];
#pragma unroll
        for (int p = 0; p < NP; ++p) {
            const unsigned LDSP* hp = (const unsigned LDSP*)(sm + ha + r * 64 + p * 512);
            A[p] = u32x4{hp[0], hp[1], hp[2], hp[3]};
            B[p] = lds16(sm + ua + r * 64 + (7 - p) * 512);
        }
#pragma unroll
        for (int p = 0; p < NP; ++p) {
            if (p <= T0) acc[0] = mfma16(A[p], B[T0 - p < 0 ? 0 : T0 - p], acc[0]);
            if (p <= T1) acc[1] = mfma16(A[p], B[T1 - p < 0 ? 0 : T1 - p], acc[1]);
            if (p <= T2) acc[2] = mfma16(A[p], B[T2 - p < 0 ? 0 : T2 - p], acc[2]);
            if (p <= T3) acc[3] = mfma16(A[p], B[T3 - p], acc[3]);
        }
    }
}
template <int SET>
__device__ __forceinline__ void conv_finish(const char LDSP* sm, int ch, int lane, const f32x4 (&acc)[4], float dskip, char* yrow, u32x2 (&yo)[4]) {
    constexpr int TI[4] = {SET ? 1 : 0, SET ? 2 : 3, SET ? 5 : 4, SET ? 6 : 7};
    const int a = lane & 15, g = lane >> 4;
#pragma unroll
    for (int i = 0; i < 4; ++i) {
        const int s = 256 * TI[i] + 16 * a + 4 * g;
        const u32x2 u = *(const u32x2 LDSP*)(sm + K2_U + ch * K2_UC + (2044 - s) * 2);
        const float y0 = rbf(acc[i][0]) + rbf(bfhi(u[1]) * dskip), y1 = rbf(acc[i][1]) + rbf(bflo(u[1]) * dskip);
        const float y2 = rbf(acc[i][2]) + rbf(bfhi(u[0]) * dskip), y3 = rbf(acc[i][3]) + rbf(bflo(u[0]) * dskip);
        yo[i] = u32x2{pack2(y0, y1), pack2(y2, y3)};
    }
#pragma unroll
    for (int i = 0; i < 4; ++i) *(u32x2*)(yrow + (256 * TI[i] + 16 * a + 4 * g) * 2) = yo[i];
}

__global__ __launch_bounds__(1024) void k2_attn(const u16* __restrict__ ssmk, const u16* __restrict__ ssmD, char* __restrict__ ws) {
    __shared__ __attribute__((aligned(16))) char smem[K2_LDS];
    const char LDSP* sm = (const char LDSP*)smem;
    char LDSP* smw = (char LDSP*)smem;
    const unsigned lds0 = (unsigned)(size_t)sm;
    const int lane = threadIdx.x & 63;
    const int w = __builtin_amdgcn_readfirstlane(threadIdx.x >> 6);
    const int bid = blockIdx.x;
    const int token = bid * 8 + (w & 7), range = w >> 3;

    const rsrc_t rsWS = mk_rsrc(ws, WS_G + 8192);
    const rsrc_t rsH = mk_rsrc(ssmk, DM * S_LEN * 2);
    u32x4 q; u32x2 kn, vm;
    bload16(q, rsWS, WS_Q + token * 1024 + (lane & 31) * 32 + (lane >> 5) * 16);
    bload8(kn, rsWS, WS_KN + lane * 8);
    bload8(vm, rsWS, WS_KN + 512 + lane * 8);
    {
        const int hch = w >> 3, hc = (w >> 2) & 1, hp = w & 3;
        dma16(rsH, (bid * 2 + hch) * 4096 + hc * 2 + hp * 1024 + lane * 16, 0, lds0 + K2_H + (hch * 2 + hc) * K2_HC + 32 + hp * 1024);
        if (w < 8) {
            dma16(rsWS, WS_UR + (bid * 2 + (w >> 2)) * 4096 + (w & 3) * 1024 + lane * 16, 0, lds0 + K2_U + (w >> 2) * K2_UC + (w & 3) * 1024);
            const unsigned o = K2_V + (w < 4 ? w : 25 + w) * 1024;
            dma16(rsWS, lane * 16, WS_KV + o, lds0 + o);
        } else {
            const unsigned o = (w < 12 ? w - 8 : SPLIT + w - 12) * 1024;
            dma16(rsWS, lane * 16, WS_KV + o, lds0 + o);
            const unsigned o2 = K2_V + (w - 4) * 1024;
            dma16(rsWS, lane * 16, WS_KV + o2, lds0 + o2);
        }
    }
#pragma unroll
    for (int i = 0; i < 7; ++i) {
        const int n = w + 16 * i;
        unsigned o;
        if (n < 56) o = (n < 24 ? 4 + n : 8 + n) * 1024;
        else { const int m = n < 108 ? n - 56 : 51; o = K2_V + (m < 17 ? 12 + m : 16 + m) * 1024; }
        dma16(rsWS, lane * 16, WS_KV + o, lds0 + o);
    }
    asm volatile("s_waitcnt vmcnt(7)" : "+v"(q), "+v"(kn), "+v"(vm) :: "memory");
    barrier();

    bool fast;
    {
        float kk = fmaxf(u2f(kn[0]), u2f(kn[1]));
        float vx = fmaxf(u2f(vm[0]), u2f(vm[1]));
        bool bad = !(u2f(kn[0]) <= 3.0e38f) || !(u2f(kn[1]) <= 3.0e38f);
#pragma unroll
        for (int o = 1; o < 64; o <<= 1) { kk = fmaxf(kk, __shfl_xor(kk, o)); vx = fmaxf(vx, __shfl_xor(vx, o)); }
        float qq = 0.f;
#pragma unroll
        for (int i = 0; i < 4; ++i) qq += bflo(q[i]) * bflo(q[i]) + bfhi(q[i]) * bfhi(q[i]);
        qq += __shfl_xor(qq, 32);
        fast = __all(!bad && qq * kk <= 3600.f && vx <= 1.0e12f);
    }

    const int isconv = w < 4;
    f32x4 cacc[4];
    if (isconv) {
        const int ch = w >> 1;
        const u16 h0 = *(const u16 LDSP*)(sm + K2_H + (ch * 2) * K2_HC + 32);
        if (lane < 16) {
            *(u16 LDSP*)(smw + K2_H + (ch * 2) * K2_HC + lane * 2) = 0;
            *(u16 LDSP*)(smw + K2_H + (ch * 2 + 1) * K2_HC + lane * 2) = lane == 15 ? h0 : (u16)0;
        }
        if (lane < 32) *(u32x4 LDSP*)(smw + K2_U + ch * K2_UC + 4096 + lane * 16) = u32x4{0, 0, 0, 0};
        wait_lgkm0();
        const float dskip = bflo((unsigned)ssmD[bid * 2 + ch]);
        char* yrow = ws + WS_YT + (size_t)(bid * 2 + ch) * 4096;
        u32x2 yo[4];
        if (w & 1) conv_wave<1>(sm, ch, lane, cacc); else conv_wave<0>(sm, ch, lane, cacc);
        wait_vm<0>();
        if (w & 1) conv_finish<1>(sm, ch, lane, cacc, dskip, yrow, yo); else conv_finish<0>(sm, ch, lane, cacc, dskip, yrow, yo);
        wait_lgkm0();
        barrier();
    }

    const int r = lane & 31, h = lane >> 5;
    const int t0 = range ? SPLIT : 0;
    const unsigned kaddr = K2_K + t0 * 1024 + lane * 16;
    int slot;
    if (r < 4) slot = r; else if (r >= 24 && r < 28) slot = r - 20; else if (r >= 12 && r < 16) slot = r - 4;
    else if (r >= 20 && r < 24) slot = r - 8; else slot = -1;
    const unsigned vrel = slot >= 0 ? (h * 16 + slot) * 16 : ((r == 4 || r == 8) ? 32 * 16 : 33 * 16);
    const unsigned vaddr = K2_V + t0 * 1088 + vrel;
    f32x16 O = {};
    float m = 0.f;
    const int dobar = w >> 2;
    if (fast) attn_fast(O, q, lds0 + kaddr, lds0 + vaddr, range, dobar);
    else { m = -__builtin_inff(); attn_slow(O, m, q, sm, kaddr, vaddr, range ? 64 - SPLIT : SPLIT, dobar); }

    f32x4 oa, ob; float l;
    if (h == 0) { oa = f32x4{O[0], O[1], O[2], O[3]}; ob = f32x4{O[12], O[13], O[14], O[15]}; l = O[4]; }
    else        { oa = f32x4{O[4], O[5], O[6], O[7]}; ob = f32x4{O[8], O[9], O[10], O[11]};   l = O[0]; }
    char LDSP* pb = smw + K2_PART + (w & 7) * 2560;
    if (range == 0) {
        *(f32x4 LDSP*)(pb + lane * 16) = oa;
        *(f32x4 LDSP*)(pb + 1024 + lane * 16) = ob;
        *(f32x2 LDSP*)(pb + 2048 + lane * 8) = f32x2{l, m};
        wait_lgkm0();
    }
    barrier();
    if (range == 1) {
        const f32x4 pa = *(const f32x4 LDSP*)(pb + lane * 16), pbb = *(const f32x4 LDSP*)(pb + 1024 + lane * 16);
        const f32x2 lm = *(const f32x2 LDSP*)(pb + 2048 + lane * 8);
        float s0 = 1.f, s1 = 1.f;
        if (!fast) { const float mn = fmaxf(m, lm[1]); s0 = __builtin_amdgcn_exp2f(lm[1] - mn); s1 = __builtin_amdgcn_exp2f(m - mn); }
        const float inv = 1.f / (lm[0] * s0 + l * s1);
        const float i0 = s0 * inv, i1 = s1 * inv;
        u32x4 o;
        o[0] = pack2(pa[0] * i0 + oa[0] * i1, pa[1] * i0 + oa[1] * i1);
        o[1] = pack2(pa[2] * i0 + oa[2] * i1, pa[3] * i0 + oa[3] * i1);
        o[2] = pack2(pbb[0] * i0 + ob[0] * i1, pbb[1] * i0 + ob[1] * i1);
        o[3] = pack2(pbb[2] * i0 + ob[2] * i1, pbb[3] * i0 + ob[3] * i1);
        *(u32x4*)(ws + WS_O + (size_t)token * 1024 + r * 32 + h * 16) = o;
    }
}

constexpr unsigned K3_YS = 131072, K3_LDS = K3_YS + 8192;

__global__ __launch_bounds__(512) void k3_out(const u16* __restrict__ x, const u16* __restrict__ wo, const char* __restrict__ ws, u16* __restrict__ out) {
    __shared__ __attribute__((aligned(16))) char smem[K3_LDS];
    const char LDSP* sm = (const char LDSP*)smem;
    const unsigned lds0 = (unsigned)(size_t)sm;
    const int lane = threadIdx.x & 63;
    const int w = __builtin_amdgcn_readfirstlane(threadIdx.x >> 6);
    const int bid = blockIdx.x;
    const int tt = (bid & 7) * 4 + (bid >> 6), j = (bid >> 3) & 7;
    const int wm = w & 1, wn = (w >> 1) & 1;

    const rsrc_t rsWS = mk_rsrc(ws, WS_G + 8192), rsO = mk_rsrc(ws + WS_O, S_LEN * DM * 2);
    const rsrc_t rsW = mk_rsrc(wo, DM * DM * 2), rsX = mk_rsrc(x, S_LEN * DM * 2);
    const int token = tt * 64 + 32 * wm + (lane & 31);
    const unsigned col = j * 64 + 32 * wn + 8 * (lane >> 5);
    u32x4 x0, x1; unsigned gi;
    bload16(x0, rsX, token * 1024 + col * 2);
    bload16(x1, rsX, token * 1024 + col * 2 + 32);
    bload4(gi, rsWS, WS_G + token * 4);
    {
        const unsigned ch = 8 * w + (lane >> 3), c = lane & 7;
        dma16(rsWS, WS_YT + (j * 64 + ch) * 4096 + tt * 128 + ((c ^ (((ch >> 1) & 1) << 2)) << 4), 0, lds0 + K3_YS + w * 1024);
    }
    gemm_issue(rsO, rsW, lds0, w, lane, tt, j);

    const GemmAddr ga = gemm_addr(wm, wn, lane);
    f32x16 acc = {};
#pragma unroll
    for (int kc = 0; kc < 8; ++kc) {
        switch (kc) {
            case 0: wait_vm<14>(); break; case 1: wait_vm<12>(); break; case 2: wait_vm<10>(); break; case 3: wait_vm<8>(); break;
            case 4: wait_vm<6>(); break;  case 5: wait_vm<4>(); break;  case 6: wait_vm<2>(); break;  default: wait_vm<0>(); break;
        }
        barrier();
        if (w < 4) gemm_chunk(sm, ga, kc, acc);
    }
    if (w < 4) {
        asm volatile("" : "+v"(x0), "+v"(x1), "+v"(gi));
        u32x2 p[4];
#pragma unroll
        for (int q = 0; q < 4; ++q) { p[q][0] = pack2(acc[4 * q], acc[4 * q + 1]); p[q][1] = pack2(acc[4 * q + 2], acc[4 * q + 3]); }
        u32x4 b0, b1;
        quads_to_rows(p, b0, b1);
        const unsigned G = lane >> 4, qq = (lane & 15) >> 2, pp = lane & 3;
        const unsigned tokc = 4 * wm + 2 * (G & 1) + (pp >> 1);
        u32x4 y[2];
#pragma unroll
        for (int k = 0; k < 2; ++k) {
            const unsigned ch0 = 32 * wn + 16 * k + 8 * (G >> 1) + qq;
            const unsigned a = K3_YS + ch0 * 128 + ((tokc ^ (((qq >> 1) & 1) << 2)) << 4) + 8 * (pp & 1);
            y[k] = cat2(ldstr(sm + a), ldstr(sm + a + 512));
        }
        const float g = __builtin_bit_cast(float, gi), g1 = rbf(1.f - g);
        u32x4 o[2];
#pragma unroll
        for (int k = 0; k < 2; ++k) {
            const u32x4 xx = k ? x1 : x0, bb = k ? b1 : b0;
#pragma unroll
            for (int i = 0; i < 4; ++i) {
                const float alo = rbf(bflo(xx[i]) * bflo(y[k][i])), ahi = rbf(bfhi(xx[i]) * bfhi(y[k][i]));
                o[k][i] = pack2(rbf(g * alo) + rbf(g1 * bflo(bb[i])), rbf(g * ahi) + rbf(g1 * bfhi(bb[i])));
            }
        }
        char* op = (char*)out + (size_t)token * 1024 + col * 2;
        *(u32x4*)op = o[0];
        *(u32x4*)(op + 32) = o[1];
    }
}

}

extern "C" void kernel_launch(void* const* d_in, const int* in_sizes, int n_in, void* d_out, int out_size, void* d_ws, size_t ws_size,
                              hipStream_t stream) {
    const u16* A_q = (const u16*)d_in[0];
    const u16* A_k = (const u16*)d_in[1];
    const u16* A_v = (const u16*)d_in[2];
    const u16* A_shift = (const u16*)d_in[3];
    const u16* A_ssmk = (const u16*)d_in[4];
    const u16* A_ssmD = (const u16*)d_in[5];
    const u16* wq = (const u16*)d_in[7];
    const u16* wk = (const u16*)d_in[8];
    const u16* wv = (const u16*)d_in[9];
    const u16* wo = (const u16*)d_in[10];
    const u16* gw = (const u16*)d_in[11];
    char* ws = (char*)d_ws;
    k1_proj<<<256, 512, 0, stream>>>(A_q, A_k, A_v, A_shift, wq, wk, wv, gw, ws);
    k2_attn<<<256, 1024, 0, stream>>>(A_ssmk, A_ssmD, ws);
    k3_out<<<256, 512, 0, stream>>>(A_q, wo, ws, (u16*)d_out);
}
```

```cpp
#include <hip/hip_runtime.h>
#include <hip/hip_bf16.h>
#include <stdint.h>

namespace {

typedef unsigned short u16;
typedef __attribute__((ext_vector_type(8))) __bf16 bf16x8;
typedef __attribute__((ext_vector_type(16))) __bf16 bf16x16;
typedef __attribute__((ext_vector_type(2))) __bf16 bf16x2;
typedef __attribute__((ext_vector_type(4))) short s16x4;
typedef __attribute__((ext_vector_type(2))) float f32x2;
typedef __attribute__((ext_vector_type(4))) float f32x4;
typedef __attribute__((ext_vector_type(8))) float f32x8;
typedef __attribute__((ext_vector_type(16))) float f32x16;
typedef __attribute__((ext_vector_type(2))) unsigned u32x2;
typedef __attribute__((ext_vector_type(4))) unsigned u32x4;
typedef __amdgpu_buffer_rsrc_t rsrc_t;
#define LDSP __attribute__((address_space(3)))

constexpr int S_LEN = 2048, DM = 512;

constexpr unsigned WS_Q  = 0;
constexpr unsigned WS_UR = 2u << 20;
constexpr unsigned WS_O  = 4u << 20;
constexpr unsigned WS_YT = 6u << 20;
constexpr unsigned WS_KV = 8u << 20;
constexpr unsigned KF_BYTES = 65536, VF_BYTES = 64 * 1088, KV_BYTES = KF_BYTES + VF_BYTES;
constexpr unsigned WS_KN = WS_KV + KV_BYTES;
constexpr unsigned WS_G  = WS_KN + 1024;
constexpr unsigned WS_FY = WS_G + 8192;
constexpr unsigned WS_WO = 9u << 20;
constexpr unsigned WS_END = WS_WO + (512u << 10);
constexpr unsigned POISON = 0xffffffffu;

#define STAMP_DECL
#define STAMP(i)
#define STAMP_FLUSH(k)
constexpr float QSCALE = 0.25f * 1.4426950408889634f;

__device__ __forceinline__ rsrc_t mk_rsrc(const void* p, unsigned bytes) {
    return __builtin_amdgcn_make_buffer_rsrc((void*)p, (short)0, (int)bytes, 0x00020000);
}
__device__ __forceinline__ void dma16(rsrc_t rs, unsigned voff, unsigned soff, unsigned ldsaddr) {
    asm volatile("s_nop 4\n\ts_mov_b32 m0, %0\n\ts_nop 0\n\tbuffer_load_dwordx4 %1, %2, %3 offen lds"
                 :: "s"(ldsaddr), "v"(voff), "s"(rs), "s"(soff) : "memory");
}
__device__ __forceinline__ void bload16(u32x4& dst, rsrc_t rs, unsigned voff) {
    asm volatile("buffer_load_dwordx4 %0, %1, %2, 0 offen" : "=v"(dst) : "v"(voff), "s"(rs) : "memory");
}
__device__ __forceinline__ void bload16_sc1(u32x4& dst, rsrc_t rs, unsigned voff) {
    asm volatile("buffer_load_dwordx4 %0, %1, %2, 0 offen sc1" : "=v"(dst) : "v"(voff), "s"(rs) : "memory");
}
__device__ __forceinline__ void bload8(u32x2& dst, rsrc_t rs, unsigned voff) {
    asm volatile("buffer_load_dwordx2 %0, %1, %2, 0 offen" : "=v"(dst) : "v"(voff), "s"(rs) : "memory");
}
__device__ __forceinline__ void bload4(unsigned& dst, rsrc_t rs, unsigned voff) {
    asm volatile("buffer_load_dword %0, %1, %2, 0 offen" : "=v"(dst) : "v"(voff), "s"(rs) : "memory");
}
__device__ __forceinline__ void st16_sc1(rsrc_t rs, unsigned voff, u32x4 v) { __builtin_amdgcn_raw_buffer_store_b128(v, rs, voff, 0, 16); }
__device__ __forceinline__ void st8_sc1(rsrc_t rs, unsigned voff, u32x2 v) { __builtin_amdgcn_raw_buffer_store_b64(v, rs, voff, 0, 16); }
__device__ __forceinline__ u32x4 ld16_sc1(rsrc_t rs, unsigned voff) { return __builtin_amdgcn_raw_buffer_load_b128(rs, voff, 0, 16); }
template <int N> __device__ __forceinline__ void wait_vm() { asm volatile("s_waitcnt vmcnt(%0)" :: "i"(N) : "memory"); }
__device__ __forceinline__ void wait_lgkm0() { asm volatile("s_waitcnt lgkmcnt(0)" ::: "memory"); }
__device__ __forceinline__ void barrier() { asm volatile("" ::: "memory"); __builtin_amdgcn_s_barrier(); asm volatile("" ::: "memory"); }

__device__ __forceinline__ unsigned pack2(float a, float b) {
    f32x2 v = {a, b};
    return __builtin_bit_cast(unsigned, __builtin_convertvector(v, bf16x2));
}
__device__ __forceinline__ float u2f(unsigned u) { return __builtin_bit_cast(float, u); }
__device__ __forceinline__ float bflo(unsigned u) { return __builtin_bit_cast(float, u << 16); }
__device__ __forceinline__ float bfhi(unsigned u) { return __builtin_bit_cast(float, u & 0xffff0000u); }
__device__ __forceinline__ float rbf(float a) { return bflo(pack2(a, 0.f)); }

__device__ __forceinline__ u32x4 lds16(const char LDSP* p) { return *(const u32x4 LDSP*)p; }
__device__ __forceinline__ u32x2 ldstr(const char LDSP* p) {
    return __builtin_bit_cast(u32x2, __builtin_amdgcn_ds_read_tr16_b64_v4i16((s16x4 LDSP*)p));
}
__device__ __forceinline__ bf16x8 asbf(u32x4 v) { return __builtin_bit_cast(bf16x8, v); }
__device__ __forceinline__ u32x4 cat2(u32x2 a, u32x2 b) { return u32x4{a[0], a[1], b[0], b[1]}; }
__device__ __forceinline__ f32x16 mfma32(u32x4 a, u32x4 b, f32x16 c) { return __builtin_amdgcn_mfma_f32_32x32x16_bf16(asbf(a), asbf(b), c, 0, 0, 0); }
__device__ __forceinline__ f32x4 mfma16(u32x4 a, u32x4 b, f32x4 c) { return __builtin_amdgcn_mfma_f32_16x16x32_bf16(asbf(a), asbf(b), c, 0, 0, 0); }

constexpr unsigned G_XS = 0, G_WS = 65536;

struct GemmAddr { unsigned xa[4]; unsigned wa; };
__device__ __forceinline__ GemmAddr gemm_addr(int wm, int wn, int lane) {
    GemmAddr g;
    const unsigned row = 32 * wm + (lane & 31), h = lane >> 5;
#pragma unroll
    for (int s = 0; s < 4; ++s) g.xa[s] = G_XS + row * 128 + (((2 * s + h) ^ ((row >> 1) & 7)) << 4);
    const unsigned G = lane >> 4, q = (lane & 15) >> 2, p = lane & 3;
    const unsigned c = 4 * wn + 2 * (G & 1) + (p >> 1);
    g.wa = G_WS + (8 * (G >> 1) + q) * 128 + ((c ^ (((q >> 1) & 1) << 2)) << 4) + 8 * (p & 1);
    return g;
}
__device__ __forceinline__ void gemm_chunk(const char LDSP* sm, const GemmAddr& g, int kc, f32x16& acc) {
    u32x4 xb[4]; u32x2 w0[4], w1[4];
#pragma unroll
    for (int s = 0; s < 4; ++s) {
        xb[s] = lds16(sm + g.xa[s] + kc * 8192);
        w0[s] = ldstr(sm + g.wa + kc * 8192 + s * 2048);
        w1[s] = ldstr(sm + g.wa + kc * 8192 + s * 2048 + 512);
    }
#pragma unroll
    for (int s = 0; s < 4; ++s) acc = mfma32(cat2(w0[s], w1[s]), xb[s], acc);
}
__device__ __forceinline__ void gemm_stage(const char LDSP* sm, const GemmAddr& g, int kc0, f32x16& acc) {
    u32x4 xb[8]; u32x2 w0[8], w1[8];
#pragma unroll
    for (int s = 0; s < 8; ++s) {
        const int kc = kc0 + (s >> 2), ss = s & 3;
        xb[s] = lds16(sm + g.xa[ss] + kc * 8192);
        w0[s] = ldstr(sm + g.wa + kc * 8192 + ss * 2048);
        w1[s] = ldstr(sm + g.wa + kc * 8192 + ss * 2048 + 512);
    }
#pragma unroll
    for (int s = 0; s < 8; ++s) acc = mfma32(cat2(w0[s], w1[s]), xb[s], acc);
}
__device__ __forceinline__ void quads_to_rows(u32x2 (&p)[4], u32x4& r0, u32x4& r1) {
    auto s0 = __builtin_amdgcn_permlane32_swap(p[0][0], p[1][0], false, false);
    auto s1 = __builtin_amdgcn_permlane32_swap(p[0][1], p[1][1], false, false);
    auto s2 = __builtin_amdgcn_permlane32_swap(p[2][0], p[3][0], false, false);
    auto s3 = __builtin_amdgcn_permlane32_swap(p[2][1], p[3][1], false, false);
    r0 = u32x4{s0[0], s1[0], s0[1], s1[1]};
    r1 = u32x4{s2[0], s3[0], s2[1], s3[1]};
}
__device__ __forceinline__ unsigned wave_umax(unsigned v) {
    v = max(v, (unsigned)__builtin_amdgcn_update_dpp(0, (int)v, 0x111, 0xf, 0xf, true));
    v = max(v, (unsigned)__builtin_amdgcn_update_dpp(0, (int)v, 0x112, 0xf, 0xf, true));
    v = max(v, (unsigned)__builtin_amdgcn_update_dpp(0, (int)v, 0x114, 0xf, 0xf, true));
    v = max(v, (unsigned)__builtin_amdgcn_update_dpp(0, (int)v, 0x118, 0xf, 0xf, true));
    v = max(v, (unsigned)__builtin_amdgcn_update_dpp(0, (int)v, 0x142, 0xa, 0xf, false));
    v = max(v, (unsigned)__builtin_amdgcn_update_dpp(0, (int)v, 0x143, 0xc, 0xf, false));
    return (unsigned)__builtin_amdgcn_readlane((int)v, 63);
}

__device__ __forceinline__ unsigned lds_ld(const char LDSP* p) { return *(const volatile unsigned LDSP*)p; }
__device__ __forceinline__ void lds_wait(const char LDSP* p, unsigned n) {
    while ((unsigned)__builtin_amdgcn_readfirstlane((int)lds_ld(p)) < n) __builtin_amdgcn_s_sleep(1);
    asm volatile("" ::: "memory");
}
__device__ __forceinline__ void lds_post(char LDSP* p, int lane) {
    asm volatile("" ::: "memory");
    if (lane == 0) __hip_atomic_fetch_add((unsigned LDSP*)p, 1u, __ATOMIC_RELAXED, __HIP_MEMORY_SCOPE_WORKGROUP);
    asm volatile("" ::: "memory");
}
__device__ __forceinline__ unsigned lds_inc(char LDSP* p, int lane) {
    unsigned old = 0;
    asm volatile("" ::: "memory");
    if (lane == 0) old = __hip_atomic_fetch_add((unsigned LDSP*)p, 1u, __ATOMIC_RELAXED, __HIP_MEMORY_SCOPE_WORKGROUP);
    old = (unsigned)__builtin_amdgcn_readfirstlane((int)old);
    asm volatile("" ::: "memory");
    return old;
}
__device__ __forceinline__ void dma1(rsrc_t rs, unsigned voff, unsigned soff, unsigned ldsaddr) {
    asm volatile("s_mov_b32 m0, %0\n\ts_nop 0\n\tbuffer_load_dwordx4 %1, %2, %3 offen lds"
                 :: "s"(ldsaddr), "v"(voff), "s"(rs), "s"(soff) : "memory");
}
__device__ __forceinline__ void dma2(rsrc_t rs, unsigned voff, unsigned soff, unsigned ldsaddr) {
    asm volatile("s_mov_b32 m0, %0\n\ts_nop 0\n\tbuffer_load_dwordx4 %1, %2, %3 offen lds\n\tbuffer_load_dwordx4 %1, %2, %3 offen offset:1024 lds"
                 :: "s"(ldsaddr), "v"(voff), "s"(rs), "s"(soff) : "memory");
}
__device__ __forceinline__ void dma4(rsrc_t rs, unsigned voff, unsigned soff, unsigned ldsaddr) {
    asm volatile("s_mov_b32 m0, %0\n\ts_nop 0\n\tbuffer_load_dwordx4 %1, %2, %3 offen lds\n\tbuffer_load_dwordx4 %1, %2, %3 offen offset:1024 lds\n\t"
                 "buffer_load_dwordx4 %1, %2, %3 offen offset:2048 lds\n\tbuffer_load_dwordx4 %1, %2, %3 offen offset:3072 lds"
                 :: "s"(ldsaddr), "v"(voff), "s"(rs), "s"(soff) : "memory");
}
__device__ __forceinline__ float dot2(unsigned a, unsigned b, float c) {
    return __builtin_amdgcn_fdot2_f32_bf16(__builtin_bit_cast(bf16x2, a), __builtin_bit_cast(bf16x2, b), c, false);
}

constexpr unsigned K1_WE = 131072;
constexpr unsigned K1_KT = K1_WE + 16384;
constexpr unsigned K1_WO = K1_KT + 8192;
constexpr unsigned K1_CNT = K1_WO + 2048;
constexpr unsigned K1_LDS = K1_CNT + 64;
enum { D_KT = 0, D_S0 = 1, D_S1 = 2, D_S2 = 3, D_S3 = 4, D_KRD = 5, D_TS = 6, D_ALL = 7 };

__global__ __launch_bounds__(512) void k1_proj(const u16* __restrict__ x, const u16* __restrict__ kin, const u16* __restrict__ vin,
                                               const u16* __restrict__ shiftk, const u16* __restrict__ wq, const u16* __restrict__ wk,
                                               const u16* __restrict__ wv, const u16* __restrict__ wo, const u16* __restrict__ gw,
                                               char* __restrict__ ws) {
    __shared__ __attribute__((aligned(16))) char smem[K1_LDS];
    const char LDSP* sm = (const char LDSP*)smem;
    char LDSP* smw = (char LDSP*)smem;
    const unsigned lds0 = (unsigned)(size_t)sm;
    const int lane = threadIdx.x & 63;
    const int w = __builtin_amdgcn_readfirstlane(threadIdx.x >> 6);
    const int bid = blockIdx.x;
    const int tt = (bid & 7) * 4 + (bid >> 6), j = (bid >> 3) & 7;
    asm volatile("" :: "s"(x), "s"(kin), "s"(vin), "s"(shiftk), "s"(wq), "s"(wk), "s"(wv), "s"(wo), "s"(gw), "s"(ws));
    const rsrc_t rsWS = mk_rsrc(ws, WS_END);
    STAMP_DECL;
    STAMP(0);
    if (w == 0) { if (lane < 16) *(unsigned LDSP*)(smw + K1_CNT + lane * 4) = 0; wait_lgkm0(); }
    barrier();

    if (w >= 4) {
        const int lw = w - 4;
        const rsrc_t rsX = mk_rsrc(x, S_LEN * DM * 2), rsW = mk_rsrc(wq, DM * DM * 2);
        const unsigned gr = lane >> 3, gc = lane & 7;
        const unsigned row0 = 16 * lw + gr;
        const unsigned vx0 = (tt * 64 + row0) * 1024 + ((gc ^ ((row0 >> 1) & 7)) << 4);
        const unsigned vx1 = (tt * 64 + row0 + 8) * 1024 + ((gc ^ (((row0 + 8) >> 1) & 7)) << 4);
        const unsigned vw0 = row0 * 1024 + j * 128 + ((gc ^ (((gr >> 1) & 1) << 2)) << 4);
        auto chunk = [&](int kc) {
            dma1(rsX, vx0, kc * 128, lds0 + G_XS + kc * 8192 + lw * 2048);
            dma1(rsX, vx1, kc * 128, lds0 + G_XS + kc * 8192 + lw * 2048 + 1024);
            dma1(rsW, vw0, kc * 65536, lds0 + G_WS + kc * 8192 + lw * 2048);
            dma1(rsW, vw0, kc * 65536 + 8192, lds0 + G_WS + kc * 8192 + lw * 2048 + 1024);
        };
        if (j < 2) {
            const rsrc_t rsE = mk_rsrc(j == 0 ? wk : wv, DM * 16 * 2);
#pragma unroll
            for (int i = 0; i < 4; ++i) {
                const unsigned prow = (lw * 4 + i) * 32 + (lane >> 1);
                const unsigned grow = prow ^ (((prow >> 3) & 1) << 2);
                dma1(rsE, grow * 32 + (lane & 1) * 16, 0, lds0 + K1_WE + (lw * 4 + i) * 1024);
            }
        } else if (j == 2) {
            if (lw == 0) dma1(mk_rsrc(gw, DM * 2), lane * 16, 0, lds0 + K1_WE);
        }
        if (lw >= 2)
            dma1(mk_rsrc(wo, DM * DM * 2), (32 * (bid & 15) + 16 * (lw - 2) + (lane >> 2)) * 1024 + (bid >> 4) * 64 + (lane & 3) * 16, 0,
                 lds0 + K1_WO + (lw - 2) * 1024);
        STAMP(4);
        chunk(0); chunk(1); chunk(2); chunk(3);
        STAMP(5);
        chunk(4); chunk(5);
        wait_vm<16>(); lds_post(smw + K1_CNT + 4 * D_S0, lane);
        STAMP(6);
        chunk(6); chunk(7);
        wait_vm<16>(); lds_post(smw + K1_CNT + 4 * D_S1, lane);
        wait_vm<8>();  lds_post(smw + K1_CNT + 4 * D_S2, lane);
        wait_vm<4>();  lds_post(smw + K1_CNT + 4 * D_S3, lane);
        wait_vm<0>();  lds_post(smw + K1_CNT + 4 * D_ALL, lane);
        STAMP(1);
        st16_sc1(rsWS, WS_O + bid * 8192 + lw * 2048 + lane * 16, u32x4{POISON, POISON, POISON, POISON});
        st16_sc1(rsWS, WS_O + bid * 8192 + lw * 2048 + 1024 + lane * 16, u32x4{POISON, POISON, POISON, POISON});
        if (lw == 0 && lane == 0) __builtin_amdgcn_raw_buffer_store_b32(0u, rsWS, WS_FY + bid * 4, 0, 16);
        lds_wait(sm + K1_CNT + 4 * D_TS, 4);
#pragma unroll
        for (int i = 0; i < 2; ++i) {
            const unsigned ch = 16 * lw + 8 * i + gr;
            st16_sc1(rsWS, WS_UR + (j * 64 + ch) * 4096 + (1984 - 64 * tt + 8 * gc) * 2, lds16(sm + K1_KT + ch * 128 + ((gc ^ (ch >> 3)) << 4)));
        }
        lds_wait(sm + K1_CNT + 4 * D_ALL, 4);
        STAMP(2);
        if (lw >= 2) {
            const unsigned G = lane >> 4, q = (lane & 15) >> 2, p = lane & 3;
            const unsigned a = K1_WO + (16 * (lw - 2) + 8 * (G >> 1) + q) * 64 + (16 * (G & 1) + 4 * p) * 2;
            st16_sc1(rsWS, WS_WO + ((bid >> 4) * 32 + 2 * (bid & 15) + (lw - 2)) * 1024 + lane * 16, cat2(ldstr(sm + a), ldstr(sm + a + 256)));
        }
        if (j < 3) {
            const unsigned row = 16 * lw + (lane & 15), g = lane >> 4;
            const unsigned ex0 = G_XS + row * 128 + ((g ^ ((row >> 1) & 7)) << 4), ex1 = G_XS + row * 128 + (((4 + g) ^ ((row >> 1) & 7)) << 4);
            const unsigned q = (lane & 15) >> 2, p = lane & 3, n = lane & 15;
            const unsigned ee0 = K1_WE + (8 * g + q + 4 * (0 ^ (g & 1))) * 32 + 8 * p, ee1 = K1_WE + (8 * g + q + 4 * (1 ^ (g & 1))) * 32 + 8 * p;
            f32x4 e0 = {}, e1 = {};
            if (j == 0) {
#pragma unroll
                for (int kc = 0; kc < 8; ++kc) {
                    e0 = mfma16(cat2(ldstr(sm + ee0 + kc * 2048), ldstr(sm + ee1 + kc * 2048)), lds16(sm + ex0 + kc * 8192), e0);
                    e1 = mfma16(cat2(ldstr(sm + ee0 + kc * 2048 + 1024), ldstr(sm + ee1 + kc * 2048 + 1024)), lds16(sm + ex1 + kc * 8192), e1);
                }
                const unsigned p0 = pack2(e0[0] + e1[0], e0[1] + e1[1]), p1 = pack2(e0[2] + e1[2], e0[3] + e1[3]);
                const int key = tt * 64 + 16 * lw + n;
                *(u32x2*)(ws + WS_KV + (key >> 5) * 1024 + (g >> 1) * 512 + (key & 31) * 16 + (g & 1) * 8) = u32x2{p0, p1};
                float ss = dot2(p0, p0, dot2(p1, p1, 0.f));
                ss += __shfl_xor(ss, 16); ss += __shfl_xor(ss, 32);
                const unsigned m = wave_umax(__builtin_bit_cast(unsigned, ss));
                if (lane == 0) ((unsigned*)(ws + WS_KN))[tt * 4 + lw] = m;
            } else if (j == 1) {
#pragma unroll
                for (int kc = 0; kc < 8; ++kc) {
                    e0 = mfma16(lds16(sm + ex0 + kc * 8192), cat2(ldstr(sm + ee0 + kc * 2048), ldstr(sm + ee1 + kc * 2048)), e0);
                    e1 = mfma16(lds16(sm + ex1 + kc * 8192), cat2(ldstr(sm + ee0 + kc * 2048 + 1024), ldstr(sm + ee1 + kc * 2048 + 1024)), e1);
                }
                const unsigned p0 = pack2(e0[0] + e1[0], e0[1] + e1[1]), p1 = pack2(e0[2] + e1[2], e0[3] + e1[3]);
                const int tile = 2 * tt + (lw >> 1), pp = lw & 1;
                char* vb = ws + WS_KV + KF_BYTES + tile * 1088 + pp * 544;
                *(u32x2*)(vb + ((g >> 1) * 16 + n) * 16 + (g & 1) * 8) = u32x2{p0, p1};
                if (lane < 2) {
                    const unsigned f = lane == 0 ? 0x3f803f80u : 0u;
                    *(u32x4*)(vb + (32 + lane) * 16) = u32x4{f, f, f, f};
                }
                const unsigned a0 = p0 & 0x7fff7fffu, a1 = p1 & 0x7fff7fffu;
                const unsigned m = wave_umax(max(max(a0 & 0xffffu, a0 >> 16), max(a1 & 0xffffu, a1 >> 16)));
                if (lane == 0) ((unsigned*)(ws + WS_KN))[128 + tt * 4 + lw] = m << 16;
            } else {
#pragma unroll
                for (int kc = 0; kc < 8; ++kc) {
                    e0 = mfma16(lds16(sm + ex0 + kc * 8192), lds16(sm + K1_WE + g * 16 + kc * 128), e0);
                    e1 = mfma16(lds16(sm + ex1 + kc * 8192), lds16(sm + K1_WE + g * 16 + kc * 128 + 64), e1);
                }
                if (n == 0) {
                    f32x4 gv;
#pragma unroll
                    for (int r = 0; r < 4; ++r) gv[r] = rbf(1.f / (1.f + __expf(-rbf(e0[r] + e1[r]))));
                    *(f32x4*)(ws + WS_G + (tt * 64 + 16 * lw + 4 * g) * 4) = gv;
                }
            }
        }
        STAMP(3);
    } else {
        const rsrc_t rsV = mk_rsrc(vin, S_LEN * DM * 2), rsS = mk_rsrc(shiftk, DM * 4 * 2);
        const int oct = lane & 7, t0 = 16 * w + 2 * (lane >> 3);
        u32x4 va, vb, sk0, sk1, sk2, sk3;
        u32x4 R0, R1, R2, R3, R4;
        {
            const rsrc_t rsK = mk_rsrc(kin, S_LEN * DM * 2);
            const unsigned vk = (unsigned)((tt * 64 - 3 + t0) * 1024 + j * 128 + oct * 16);
            bload16(R0, rsK, vk); bload16(R1, rsK, vk + 1024); bload16(R2, rsK, vk + 2048); bload16(R3, rsK, vk + 3072); bload16(R4, rsK, vk + 4096);
        }
        bload16(va, rsV, (tt * 64 + t0) * 1024 + j * 128 + oct * 16);
        bload16(vb, rsV, (tt * 64 + t0 + 1) * 1024 + j * 128 + oct * 16);
        {
            const unsigned so = (j * 64 + oct * 8) * 8;
            bload16(sk0, rsS, so); bload16(sk1, rsS, so + 16); bload16(sk2, rsS, so + 32); bload16(sk3, rsS, so + 48);
        }
        const int wm = w & 1, wn = w >> 1;
        const GemmAddr ga = gemm_addr(wm, wn, lane);
        STAMP(6);
        asm volatile("s_waitcnt vmcnt(0)" : "+v"(va), "+v"(vb), "+v"(sk0), "+v"(sk1), "+v"(sk2), "+v"(sk3), "+v"(R0), "+v"(R1), "+v"(R2), "+v"(R3), "+v"(R4) :: "memory");
        STAMP(1);
        {
            const u32x4 R[5] = {R0, R1, R2, R3, R4};
            const u32x4 sk[4] = {sk0, sk1, sk2, sk3};
            unsigned tw[8];
#pragma unroll
            for (int c = 0; c < 8; ++c) {
                const int d = c >> 1;
                const unsigned sel = (c & 1) ? 0x07060302u : 0x05040100u;
                const unsigned s01 = sk[d][(c & 1) * 2], s23 = sk[d][(c & 1) * 2 + 1];
                const float a0 = dot2(__builtin_amdgcn_perm(R[2][d], R[3][d], sel), s01, dot2(__builtin_amdgcn_perm(R[0][d], R[1][d], sel), s23, 0.f));
                const float a1 = dot2(__builtin_amdgcn_perm(R[3][d], R[4][d], sel), s01, dot2(__builtin_amdgcn_perm(R[1][d], R[2][d], sel), s23, 0.f));
                const float v0 = (c & 1) ? bfhi(va[d]) : bflo(va[d]), v1 = (c & 1) ? bfhi(vb[d]) : bflo(vb[d]);
                tw[c] = pack2(a1 * v1, a0 * v0);
            }
            const unsigned mp = 62 - t0;
#pragma unroll
            for (int c = 0; c < 8; ++c)
                *(unsigned LDSP*)(smw + K1_KT + (8 * oct + c) * 128 + (((mp >> 3) ^ oct) << 4) + (mp & 7) * 2) = tw[c];
            wait_lgkm0();
            lds_post(smw + K1_CNT + 4 * D_TS, lane);
        }
        STAMP(2);
        f32x16 acc = {};
        lds_wait(sm + K1_CNT + 4 * D_S0, 4); gemm_stage(sm, ga, 0, acc);
        lds_wait(sm + K1_CNT + 4 * D_S1, 4); gemm_stage(sm, ga, 2, acc);
        STAMP(3);
        lds_wait(sm + K1_CNT + 4 * D_S2, 4); gemm_stage(sm, ga, 4, acc);
        lds_wait(sm + K1_CNT + 4 * D_S3, 4); gemm_chunk(sm, ga, 6, acc);
        lds_wait(sm + K1_CNT + 4 * D_ALL, 4); gemm_chunk(sm, ga, 7, acc);
        STAMP(4);
        u32x2 p[4];
#pragma unroll
        for (int q = 0; q < 4; ++q) {
            const unsigned a = pack2(acc[4 * q], acc[4 * q + 1]), b = pack2(acc[4 * q + 2], acc[4 * q + 3]);
            p[q][0] = pack2(bflo(a) * QSCALE, bfhi(a) * QSCALE);
            p[q][1] = pack2(bflo(b) * QSCALE, bfhi(b) * QSCALE);
        }
        u32x4 r0, r1;
        quads_to_rows(p, r0, r1);
        const unsigned qo = WS_Q + (tt * 64 + 32 * wm + (lane & 31)) * 1024 + (j * 64 + 32 * wn + 8 * (lane >> 5)) * 2;
        st16_sc1(rsWS, qo, r0);
        st16_sc1(rsWS, qo + 32, r1);
        STAMP(5);
    }
    STAMP_FLUSH(0);
}

constexpr unsigned K2_K = 0, K2_V = 65536, K2_H = KV_BYTES, K2_HC = 4160, K2_U = K2_H + 4 * K2_HC, K2_UC = 4608;
constexpr unsigned K2_CNT = K2_U + 2 * K2_UC;
constexpr unsigned K2_LDS = K2_CNT + 64;
constexpr unsigned K2_XP = K2_H, K2_YP = K2_H + 8192;
constexpr unsigned K2_PART = 0;
constexpr unsigned K2_OI = 32768;
#define XSLEEP 10
enum { C_ST0 = 0, C_ST1 = 1, C_ST2 = 2, C_ST3 = 3, C_CH0 = 4, C_CH1 = 5, C_YST = 6, C_SLOW = 8   };

#define AE(X, i) "v_exp_f32 v[" #X "+" #i "], v[" #X "+" #i "]\n\t"
#define ASTEP(X0, X1, Y0, Y1, t)                                                             \
    AE(X0, 0) AE(X0, 1) AE(X0, 2) AE(X0, 3)                                                  \
    "s_waitcnt lgkmcnt(2)\n\t"                                                               \
    "v_mfma_f32_32x32x16_bf16 v[" #Y0 ":" #Y0 "+15], %2, %10, 0\n\t"                         \
    AE(X0, 4) AE(X0, 5) AE(X0, 6) AE(X0, 7)                                                  \
    "v_mfma_f32_32x32x16_bf16 v[" #Y1 ":" #Y1 "+15], %2, %11, 0\n\t"                         \
    "ds_read_b128 %2, %5 offset:(" #t "+2)*1024\n\t"                                         \
    AE(X0, 8) AE(X0, 9) AE(X0, 10) AE(X0, 11)                                                \
    "s_waitcnt lgkmcnt(2)\n\t"                                                               \
    "v_smfmac_f32_32x32x32_bf16 %0, %3, v[" #X0 ":" #X0 "+7], %12\n\t"                       \
    AE(X0, 12) AE(X0, 13) AE(X0, 14) AE(X0, 15) AE(X1, 0) AE(X1, 1)                          \
    "s_waitcnt lgkmcnt(1)\n\t"                                                               \
    "v_smfmac_f32_32x32x32_bf16 %0, %4, v[" #X0 "+8:" #X0 "+15], %12\n\t"                    \
    AE(X1, 2) AE(X1, 3) AE(X1, 4) AE(X1, 5) AE(X1, 6) AE(X1, 7) AE(X1, 8) AE(X1, 9)          \
    "v_smfmac_f32_32x32x32_bf16 %1, %3, v[" #X1 ":" #X1 "+7], %12\n\t"                       \
    "ds_read_b128 %3, %6 offset:(" #t "+1)*1088\n\t"                                         \
    AE(X1, 10) AE(X1, 11) AE(X1, 12) AE(X1, 13) AE(X1, 14) AE(X1, 15)                        \
    "s_nop 1\n\t"                                                                            \
    "v_smfmac_f32_32x32x32_bf16 %1, %4, v[" #X1 "+8:" #X1 "+15], %12\n\t"                    \
    "ds_read_b128 %4, %6 offset:(" #t "+1)*1088+544\n\t"
#define ASTEP_E(t) ASTEP(0, 16, 32, 48, t)
#define ASTEP_O(t) ASTEP(32, 48, 0, 16, t)
#define AWAIT(lbl, off)                                                                      \
    #lbl ":\n\t"                                                                             \
    "ds_read_b32 %7, %13 offset:" #off "\n\t"                                                \
    "s_waitcnt lgkmcnt(0)\n\t"                                                               \
    "v_readfirstlane_b32 %8, %7\n\t"                                                         \
    "s_nop 3\n\t"                                                                            \
    "s_cmp_ge_u32 %8, 4\n\t"                                                                 \
    "s_cbranch_scc1 " #lbl "9f\n\t"                                                          \
    "s_sleep 1\n\t"                                                                          \
    "s_branch " #lbl "b\n\t"                                                                 \
    #lbl "9:\n\t"

__device__ __forceinline__ void attn_fast(f32x16& O0, f32x16& O1, u32x4 q0, u32x4 q1, unsigned kaddr, unsigned vaddr, unsigned caddr) {
    u32x4 kf, v0, v1;
    unsigned idx = 0xddddu, tv; int ts, it = 0;
    asm volatile(
        "s_setprio 2\n\t"
        AWAIT(1, 0)
        "ds_read_b128 %2, %5\n\t"
        "s_waitcnt lgkmcnt(0)\n\t"
        "v_mfma_f32_32x32x16_bf16 v[0:15], %2, %10, 0\n\t"
        "v_mfma_f32_32x32x16_bf16 v[16:31], %2, %11, 0\n\t"
        "ds_read_b128 %2, %5 offset:1024\n\t"
        "ds_read_b128 %3, %6\n\t"
        "ds_read_b128 %4, %6 offset:544\n\t"
        "s_nop 10\n\t"
        "4:\n\t"
        ASTEP_E(0) ASTEP_O(1)
        "s_cmp_gt_u32 %9, 1\n\t"
        "s_cbranch_scc1 5f\n\t"
        "s_cmp_eq_u32 %9, 0\n\t"
        "s_cbranch_scc0 7f\n\t"
        AWAIT(2, 4)
        "s_branch 5f\n\t"
        "7:\n\t"
        AWAIT(8, 12)
        "5:\n\t"
        ASTEP_E(2) ASTEP_O(3) ASTEP_E(4) ASTEP_O(5)
        "s_cmp_lg_u32 %9, 0\n\t"
        "s_cbranch_scc1 6f\n\t"
        AWAIT(3, 8)
        "6:\n\t"
        ASTEP_E(6) ASTEP_O(7)
        "v_add_u32 %5, 0x2000, %5\n\t"
        "v_add_u32 %6, 0x2200, %6\n\t"
        "s_add_u32 %9, %9, 1\n\t"
        "s_cmp_eq_u32 %9, 2\n\t"
        "s_cbranch_scc0 10f\n\t"
        "s_setprio 1\n\t"
        "10:\n\t"
        "s_cmp_eq_u32 %9, 3\n\t"
        "s_cbranch_scc0 11f\n\t"
        "s_setprio 0\n\t"
        "11:\n\t"
        "s_cmp_lg_u32 %9, 4\n\t"
        "s_cbranch_scc1 4b\n\t"
        "s_waitcnt lgkmcnt(0)\n\t"
        "s_nop 15\n\t"
        "s_nop 7\n\t"
        : "+v"(O0), "+v"(O1), "=&v"(kf), "=&v"(v0), "=&v"(v1), "+v"(kaddr), "+v"(vaddr), "=&v"(tv), "=&s"(ts), "+s"(it)
        : "v"(q0), "v"(q1), "v"(idx), "v"(caddr)
        : "v0", "v1", "v2", "v3", "v4", "v5", "v6", "v7", "v8", "v9", "v10", "v11", "v12", "v13", "v14", "v15",
          "v16", "v17", "v18", "v19", "v20", "v21", "v22", "v23", "v24", "v25", "v26", "v27", "v28", "v29", "v30", "v31",
          "v32", "v33", "v34", "v35", "v36", "v37", "v38", "v39", "v40", "v41", "v42", "v43", "v44", "v45", "v46", "v47",
          "v48", "v49", "v50", "v51", "v52", "v53", "v54", "v55", "v56", "v57", "v58", "v59", "v60", "v61", "v62", "v63",
          "scc", "memory");
}

__device__ __forceinline__ void attn_slow(f32x16& O, float& m, u32x4 q, const char LDSP* sm, unsigned kaddr, unsigned vaddr) {
#pragma unroll 1
    for (int t = 0; t < 32; ++t) {
        const u32x4 kf = lds16(sm + kaddr + t * 1024);
        const u32x4 v0 = lds16(sm + vaddr + t * 1088), v1 = lds16(sm + vaddr + t * 1088 + 544);
        f32x16 s = mfma32(kf, q, f32x16{});
        float tm = s[0];
#pragma unroll
        for (int r = 1; r < 16; ++r) tm = fmaxf(tm, s[r]);
        tm = fmaxf(tm, __shfl_xor(tm, 32));
        const float mn = fmaxf(m, tm);
        const float alpha = __builtin_amdgcn_exp2f(m - mn);
        m = mn;
#pragma unroll
        for (int r = 0; r < 16; ++r) { O[r] *= alpha; s[r] = __builtin_amdgcn_exp2f(s[r] - mn); }
        const f32x8 slo = __builtin_shufflevector(s, s, 0, 1, 2, 3, 4, 5, 6, 7), shi = __builtin_shufflevector(s, s, 8, 9, 10, 11, 12, 13, 14, 15);
        O = __builtin_amdgcn_smfmac_f32_32x32x32_bf16(asbf(v0), __builtin_bit_cast(bf16x16, slo), O, 0xdddd, 0, 0);
        O = __builtin_amdgcn_smfmac_f32_32x32x32_bf16(asbf(v1), __builtin_bit_cast(bf16x16, shi), O, 0xdddd, 0, 0);
    }
}

template <int SET>
__device__ __forceinline__ void conv_wave(const char LDSP* sm, int ch, int lane, float dskip, rsrc_t rsWS, unsigned yoff) {
    constexpr int T0 = SET ? 1 : 0, T1 = SET ? 2 : 3, T2 = SET ? 5 : 4, T3 = SET ? 6 : 7;
    constexpr int NP = T3 + 1;
    const int b = lane & 15, g = lane >> 4;
    const int c = (b + 1) & 1;
    unsigned ha[4];
#pragma unroll
    for (int i = 0; i < 4; ++i) {
        ha[i] = K2_H + (ch * 2 + c) * K2_HC + ((b + 1 - c) / 2 + 4 * g) * 4 + i * 1024;
        asm volatile("" : "+v"(ha[i]));
    }
    unsigned ua = K2_U + ch * K2_UC + (2032 - 16 * b + 8 * g) * 2 - 7 * 512;
    asm volatile("" : "+v"(ua));
    f32x4 acc[4] = {};
#pragma unroll
    for (int r = 0; r < 8; ++r) {
        u32x4 A[NP], B[NP];
#pragma unroll
        for (int p = 0; p < NP; ++p) {
            const unsigned LDSP* hp = (const unsigned LDSP*)(sm + ha[p >> 1] + r * 64 + (p & 1) * 512);
            A[p] = u32x4{hp[0], hp[1], hp[2], hp[3]};
            B[p] = lds16(sm + ua + r * 64 + (7 - p) * 512);
        }
#pragma unroll
        for (int p = 0; p < NP; ++p) {
            if (p <= T0) acc[0] = mfma16(A[p], B[T0 - p < 0 ? 0 : T0 - p], acc[0]);
            if (p <= T1) acc[1] = mfma16(A[p], B[T1 - p < 0 ? 0 : T1 - p], acc[1]);
            if (p <= T2) acc[2] = mfma16(A[p], B[T2 - p < 0 ? 0 : T2 - p], acc[2]);
            if (p <= T3) acc[3] = mfma16(A[p], B[T3 - p], acc[3]);
        }
    }
    constexpr int TI[4] = {T0, T1, T2, T3};
#pragma unroll
    for (int i = 0; i < 4; ++i) {
        const int s = 256 * TI[i] + 16 * b + 4 * g;
        const u32x2 u = *(const u32x2 LDSP*)(sm + K2_U + ch * K2_UC + (2044 - s) * 2);
        const float y0 = rbf(acc[i][0]) + rbf(bfhi(u[1]) * dskip), y1 = rbf(acc[i][1]) + rbf(bflo(u[1]) * dskip);
        const float y2 = rbf(acc[i][2]) + rbf(bfhi(u[0]) * dskip), y3 = rbf(acc[i][3]) + rbf(bflo(u[0]) * dskip);
        st8_sc1(rsWS, yoff + s * 2, u32x2{pack2(y0, y1), pack2(y2, y3)});
    }
}

template <int I0>
__device__ __forceinline__ void oproj12(f32x16& acc, const u32x4* wf, unsigned ob) {
    u32x4 t0, t1, t2, t3; unsigned a;
    asm volatile(
        "v_xor_b32 %5, (((%c19+0)&7)<<5), %18\n\t" "ds_read_b128 %1, %5 offset:((%c19+0)>>3)*256\n\t"
        "v_xor_b32 %5, (((%c19+1)&7)<<5), %18\n\t" "ds_read_b128 %2, %5 offset:((%c19+1)>>3)*256\n\t"
        "v_xor_b32 %5, (((%c19+2)&7)<<5), %18\n\t" "ds_read_b128 %3, %5 offset:((%c19+2)>>3)*256\n\t"
        "v_xor_b32 %5, (((%c19+3)&7)<<5), %18\n\t" "ds_read_b128 %4, %5 offset:((%c19+3)>>3)*256\n\t"
        "s_waitcnt lgkmcnt(3)\n\t" "v_mfma_f32_32x32x16_bf16 %0, %6, %1, %0\n\t"
        "v_xor_b32 %5, (((%c19+4)&7)<<5), %18\n\t" "ds_read_b128 %1, %5 offset:((%c19+4)>>3)*256\n\t"
        "s_waitcnt lgkmcnt(3)\n\t" "v_mfma_f32_32x32x16_bf16 %0, %7, %2, %0\n\t"
        "v_xor_b32 %5, (((%c19+5)&7)<<5), %18\n\t" "ds_read_b128 %2, %5 offset:((%c19+5)>>3)*256\n\t"
        "s_waitcnt lgkmcnt(3)\n\t" "v_mfma_f32_32x32x16_bf16 %0, %8, %3, %0\n\t"
        "v_xor_b32 %5, (((%c19+6)&7)<<5), %18\n\t" "ds_read_b128 %3, %5 offset:((%c19+6)>>3)*256\n\t"
        "s_waitcnt lgkmcnt(3)\n\t" "v_mfma_f32_32x32x16_bf16 %0, %9, %4, %0\n\t"
        "v_xor_b32 %5, (((%c19+7)&7)<<5), %18\n\t" "ds_read_b128 %4, %5 offset:((%c19+7)>>3)*256\n\t"
        "s_waitcnt lgkmcnt(3)\n\t" "v_mfma_f32_32x32x16_bf16 %0, %10, %1, %0\n\t"
        "v_xor_b32 %5, (((%c19+8)&7)<<5), %18\n\t" "ds_read_b128 %1, %5 offset:((%c19+8)>>3)*256\n\t"
        "s_waitcnt lgkmcnt(3)\n\t" "v_mfma_f32_32x32x16_bf16 %0, %11, %2, %0\n\t"
        "v_xor_b32 %5, (((%c19+9)&7)<<5), %18\n\t" "ds_read_b128 %2, %5 offset:((%c19+9)>>3)*256\n\t"
        "s_waitcnt lgkmcnt(3)\n\t" "v_mfma_f32_32x32x16_bf16 %0, %12, %3, %0\n\t"
        "v_xor_b32 %5, (((%c19+10)&7)<<5), %18\n\t" "ds_read_b128 %3, %5 offset:((%c19+10)>>3)*256\n\t"
        "s_waitcnt lgkmcnt(3)\n\t" "v_mfma_f32_32x32x16_bf16 %0, %13, %4, %0\n\t"
        "v_xor_b32 %5, (((%c19+11)&7)<<5), %18\n\t" "ds_read_b128 %4, %5 offset:((%c19+11)>>3)*256\n\t"
        "s_waitcnt lgkmcnt(3)\n\t" "v_mfma_f32_32x32x16_bf16 %0, %14, %1, %0\n\t"
        "s_waitcnt lgkmcnt(2)\n\t" "v_mfma_f32_32x32x16_bf16 %0, %15, %2, %0\n\t"
        "s_waitcnt lgkmcnt(1)\n\t" "v_mfma_f32_32x32x16_bf16 %0, %16, %3, %0\n\t"
        "s_waitcnt lgkmcnt(0)\n\t" "v_mfma_f32_32x32x16_bf16 %0, %17, %4, %0\n\t"
        "s_nop 15\n\t"
        : "+v"(acc), "=&v"(t0), "=&v"(t1), "=&v"(t2), "=&v"(t3), "=&v"(a)
        : "v"(wf[0]), "v"(wf[1]), "v"(wf[2]), "v"(wf[3]), "v"(wf[4]), "v"(wf[5]), "v"(wf[6]), "v"(wf[7]), "v"(wf[8]), "v"(wf[9]), "v"(wf[10]), "v"(wf[11]),
          "v"(ob), "i"(I0)
        : "memory");
}

__global__ __launch_bounds__(768) void k2_main(const u16* __restrict__ x, const u16* __restrict__ ssmk, const u16* __restrict__ ssmD,
                                               char* __restrict__ ws, u16* __restrict__ out) {
    __shared__ __attribute__((aligned(16))) char smem[K2_LDS];
    const char LDSP* sm = (const char LDSP*)smem;
    char LDSP* smw = (char LDSP*)smem;
    const unsigned lds0 = (unsigned)(size_t)sm;
    const int lane = threadIdx.x & 63;
    const int w = __builtin_amdgcn_readfirstlane(threadIdx.x >> 6);
    const int bid = blockIdx.x;
    const int tb = (bid & 7) * 8 + (bid >> 5), hg = (bid >> 3) & 3;
    asm volatile("" :: "s"(x), "s"(ssmk), "s"(ssmD), "s"(ws), "s"(out));
    const rsrc_t rsWS = mk_rsrc(ws, WS_END);
    const int r = lane & 31, h = lane >> 5;
    STAMP_DECL;
    STAMP(0);

    if (w < 8) {
        const int rg = w & 3, kh = w >> 2;
        u32x4 q0, q1;
        const unsigned qoff = WS_Q + (unsigned)(tb * 32 + 8 * rg + (r >> 3)) * 1024 + (8 * hg + (r & 7)) * 32 + h * 16;
        bload16(q0, rsWS, qoff + kh * 4096);
        bload16(q1, rsWS, qoff + (kh ^ 1) * 4096);
        const unsigned kaddr = K2_K + kh * 32 * 1024 + lane * 16;
        const unsigned vaddr = K2_V + kh * 32 * 1088 + (r < 16 ? (h * 16 + r) * 16 : ((r & 11) == 0 ? 32 * 16 : 33 * 16));
        barrier();
        asm volatile("s_waitcnt vmcnt(0)" : "+v"(q0), "+v"(q1) :: "memory");
        f32x16 O0 = {}, O1 = {};
        STAMP(1);
        attn_fast(O0, O1, q0, q1, lds0 + kaddr, lds0 + vaddr, lds0 + K2_CNT);
        STAMP(2);
        barrier();
        float m0 = 0.f, m1 = 0.f;
        const u32x4 slow4 = lds16(sm + K2_CNT + 4 * C_SLOW);
        const bool anyslow = __builtin_amdgcn_readfirstlane((int)(slow4[0] | slow4[1] | slow4[2] | slow4[3])) != 0;
        bool slow = false;
        if (anyslow) {
            slow = __builtin_amdgcn_readfirstlane((int)lds_ld(sm + K2_CNT + 4 * (C_SLOW + rg))) != 0;
            if (slow) {
                O0 = f32x16{}; O1 = f32x16{};
                m0 = m1 = -__builtin_inff();
                attn_slow(O0, m0, q0, sm, kaddr, vaddr);
                attn_slow(O1, m1, q1, sm, kaddr, vaddr);
            }
            barrier();
        }
        STAMP(3);
        {
            char LDSP* pw = smw + K2_PART + w * 2560;
            *(f32x4 LDSP*)(pw + lane * 16) = f32x4{O1[0], O1[1], O1[2], O1[3]};
            *(f32x4 LDSP*)(pw + 1024 + lane * 16) = f32x4{O1[4], O1[5], O1[6], O1[7]};
            *(f32x2 LDSP*)(pw + 2048 + lane * 8) = f32x2{O1[8], m1};
        }
        wait_lgkm0();
        barrier();
        STAMP(4);
        u32x4 o;
        {
            const char LDSP* pr = sm + K2_PART + (w ^ 4) * 2560;
            f32x4 pa = *(const f32x4 LDSP*)(pr + lane * 16), pb = *(const f32x4 LDSP*)(pr + 1024 + lane * 16);
            const f32x2 lm = *(const f32x2 LDSP*)(pr + 2048 + lane * 8);
            f32x4 ka = f32x4{O0[0], O0[1], O0[2], O0[3]}, kb = f32x4{O0[4], O0[5], O0[6], O0[7]};
            float l0 = O0[8], l1 = lm[0];
            if (slow) {
                const float mn = fmaxf(m0, lm[1]);
                const float s0 = __builtin_amdgcn_exp2f(m0 - mn), s1 = __builtin_amdgcn_exp2f(lm[1] - mn);
                ka *= s0; kb *= s0; l0 *= s0; pa *= s1; pb *= s1; l1 *= s1;
            }
            const float inv = __builtin_amdgcn_rcpf(l0 + l1);
            ka = (ka + pa) * inv; kb = (kb + pb) * inv;
            auto s0 = __builtin_amdgcn_permlane32_swap(pack2(ka[0], ka[1]), pack2(kb[0], kb[1]), false, false);
            auto s1 = __builtin_amdgcn_permlane32_swap(pack2(ka[2], ka[3]), pack2(kb[2], kb[3]), false, false);
            o = u32x4{min(s0[0], POISON - 1), min(s1[0], POISON - 1), min(s0[1], POISON - 1), min(s1[1], POISON - 1)};
        }
        const int tokl = 8 * rg + 4 * kh + (r >> 3);
        st16_sc1(rsWS, WS_O + (tb * 32 + tokl) * 1024 + (8 * hg + (r & 7)) * 32 + h * 16, o);
        *(u32x4 LDSP*)(smw + K2_OI + tokl * 1024 + ((((r & 7) * 2 + h) ^ (tokl & 15)) << 4)) = o;
        const int tk = 4 * w + (lane >> 4), c = lane & 15;
        const unsigned poff = WS_O + (tb * 32 + tk) * 1024 + c * 16;
        const unsigned po0 = poff + ((hg + 1) & 3) * 256, po1 = poff + ((hg + 2) & 3) * 256, po2 = poff + ((hg + 3) & 3) * 256;
        wait_lgkm0();
        barrier();
        STAMP(5);
        __builtin_amdgcn_s_sleep(XSLEEP);
        u32x4 p0, p1, p2;
        bload16_sc1(p0, rsWS, po0); bload16_sc1(p1, rsWS, po1); bload16_sc1(p2, rsWS, po2);
        asm volatile("s_waitcnt vmcnt(0)" : "+v"(p0), "+v"(p1), "+v"(p2) :: "memory");
        for (unsigned spins = 0; spins < (1u << 20); ++spins) {
            const unsigned mx = max(max(max(max(p0[0], p0[1]), max(p0[2], p0[3])), max(max(p1[0], p1[1]), max(p1[2], p1[3]))),
                                    max(max(p2[0], p2[1]), max(p2[2], p2[3])));
            if (__all(mx != POISON)) break;
            bload16_sc1(p0, rsWS, po0); bload16_sc1(p1, rsWS, po1); bload16_sc1(p2, rsWS, po2);
            asm volatile("s_waitcnt vmcnt(0)" : "+v"(p0), "+v"(p1), "+v"(p2) :: "memory");
        }
        *(u32x4 LDSP*)(smw + K2_OI + tk * 1024 + (((16 + c) ^ (tk & 15)) << 4)) = p0;
        *(u32x4 LDSP*)(smw + K2_OI + tk * 1024 + (((32 + c) ^ (tk & 15)) << 4)) = p1;
        *(u32x4 LDSP*)(smw + K2_OI + tk * 1024 + (((48 + c) ^ (tk & 15)) << 4)) = p2;
        STAMP(6);
        wait_lgkm0();
        barrier();
        STAMP(7);
    } else {
        const int hw = w - 8;
        __builtin_amdgcn_s_setprio(3);
        const unsigned ko = hw < 2 ? hw * 32768 : K2_V + (hw - 2) * 34816;
        auto group = [&](int g) { dma4(rsWS, lane * 16, WS_KV + ko + g * 4096, lds0 + ko + g * 4096); };
        group(0);
        if (hw == 0 && lane < 16) *(unsigned LDSP*)(smw + K2_CNT + lane * 4) = 0;
        wait_lgkm0();
        barrier();
        group(1);
        wait_vm<4>(); lds_post(smw + K2_CNT + 4 * C_ST0, lane);
        group(2);
        wait_vm<4>(); lds_post(smw + K2_CNT + 4 * C_ST1, lane);
        group(3);
        wait_vm<4>(); lds_post(smw + K2_CNT + 4 * C_ST2, lane);
        group(4); group(5); group(6); group(7);
        if (hw >= 2) dma2(rsWS, lane * 16, WS_KV + ko + 32768, lds0 + ko + 32768);
        const int ch = hw >> 1, st = hw & 1;
        dma4(mk_rsrc(ssmk, DM * S_LEN * 2), st * 2 + lane * 16, (bid * 2 + ch) * 4096, lds0 + K2_H + (ch * 2 + st) * K2_HC + 32);
        dma2(rsWS, lane * 16, WS_UR + (bid * 2 + ch) * 4096 + st * 2048, lds0 + K2_U + ch * K2_UC + st * 2048);
        u32x4 q0, q1; u32x2 kn, vm;
        {
            const unsigned qoff = WS_Q + (unsigned)(tb * 32 + 8 * hw + (r >> 3)) * 1024 + (8 * hg + (r & 7)) * 32 + h * 16;
            bload16(q0, rsWS, qoff); bload16(q1, rsWS, qoff + 4096);
            bload8(kn, rsWS, WS_KN + lane * 8); bload8(vm, rsWS, WS_KN + 512 + lane * 8);
        }
        wait_vm<10>(); lds_post(smw + K2_CNT + 4 * C_ST3, lane);
        wait_vm<4>();  lds_post(smw + K2_CNT + 4 * (C_CH0 + ch), lane);
        asm volatile("s_waitcnt vmcnt(0)" : "+v"(q0), "+v"(q1), "+v"(kn), "+v"(vm) :: "memory");
        {
            const float kk = u2f(wave_umax(max(kn[0], kn[1]))), vx = u2f(wave_umax(max(vm[0], vm[1])));
            float qq0 = 0.f, qq1 = 0.f;
#pragma unroll
            for (int i = 0; i < 4; ++i) { qq0 = dot2(q0[i], q0[i], qq0); qq1 = dot2(q1[i], q1[i], qq1); }
            float qq = fmaxf(qq0 + __shfl_xor(qq0, 32), qq1 + __shfl_xor(qq1, 32));
            if (!(qq0 == qq0) || !(qq1 == qq1)) qq = __builtin_inff();
            const bool fast = __all(qq * kk <= 3600.f && vx <= 1.0e12f);
            if (lane == 0) *(unsigned LDSP*)(smw + K2_CNT + 4 * (C_SLOW + hw)) = fast ? 0u : 1u;
        }
        lds_wait(sm + K2_CNT + 4 * (C_CH0 + ch), 2);
        STAMP(1);
        {
            const u16 h0 = *(const u16 LDSP*)(sm + K2_H + (ch * 2) * K2_HC + 32);
            if (lane < 16) {
                *(u16 LDSP*)(smw + K2_H + (ch * 2) * K2_HC + lane * 2) = 0;
                *(u16 LDSP*)(smw + K2_H + (ch * 2 + 1) * K2_HC + lane * 2) = lane == 15 ? h0 : (u16)0;
            }
            if (lane < 32) *(u32x4 LDSP*)(smw + K2_U + ch * K2_UC + 4096 + lane * 16) = u32x4{0, 0, 0, 0};
            wait_lgkm0();
            const float dskip = bflo((unsigned)ssmD[bid * 2 + ch]);
            const unsigned yoff = WS_YT + (bid * 2 + ch) * 4096;
            if (st) conv_wave<1>(sm, ch, lane, dskip, rsWS, yoff); else conv_wave<0>(sm, ch, lane, dskip, rsWS, yoff);
        }
        STAMP(2);
        wait_vm<0>();
        if (lds_inc(smw + K2_CNT + 4 * C_YST, lane) == 3) {
            if (lane == 0) __hip_atomic_store((unsigned*)(ws + WS_FY) + bid, 1u, __ATOMIC_RELAXED, __HIP_MEMORY_SCOPE_AGENT);
        }
        lds_wait(sm + K2_CNT + 4 * C_YST, 4);
        const int nt = 4 * hg + hw;
        {
            const rsrc_t rsX = mk_rsrc(x, S_LEN * DM * 2);
            const unsigned vxo = (tb * 32 + (lane >> 2)) * 1024 + nt * 64 + (lane & 3) * 16;
            dma1(rsX, vxo, 0, lds0 + K2_XP + hw * 2048);
            dma1(rsX, vxo, 16 * 1024, lds0 + K2_XP + hw * 2048 + 1024);
        }
        const float g = ((const float*)(ws + WS_G))[tb * 32 + r];
        u32x4 wf[32];
#pragma unroll
        for (int i = 0; i < 32; ++i) wf[i] = *(const u32x4*)(ws + WS_WO + (nt * 32 + ((8 * hg + i) & 31)) * 1024 + lane * 16);
        {
            unsigned spins = 0;
            const unsigned* fp = (const unsigned*)(ws + WS_FY) + 16 * nt + (lane & 15);
            while (!__all(__hip_atomic_load(fp, __ATOMIC_RELAXED, __HIP_MEMORY_SCOPE_AGENT) != 0) && ++spins < (1u << 20)) __builtin_amdgcn_s_sleep(8);
            asm volatile("" ::: "memory");
#pragma unroll
            for (int i = 0; i < 2; ++i) {
                const u32x4 yv = ld16_sc1(rsWS, WS_YT + (nt * 32 + 16 * i + (lane >> 2)) * 4096 + tb * 64 + (lane & 3) * 16);
                *(u32x4 LDSP*)(smw + K2_YP + hw * 2048 + i * 1024 + lane * 16) = yv;
            }
        }
        wait_vm<0>();
        wait_lgkm0();
        u32x2 xy[4];
        {
            const unsigned G = lane >> 4, qq = (lane & 15) >> 2, pp = lane & 3;
#pragma unroll
            for (int q = 0; q < 4; ++q) {
                const u32x2 y = ldstr(sm + K2_YP + hw * 2048 + (8 * q + 4 * (G >> 1) + qq) * 64 + (16 * (G & 1) + 4 * pp) * 2);
                const u32x2 xx = *(const u32x2 LDSP*)(sm + K2_XP + hw * 2048 + r * 64 + (8 * q + 4 * h) * 2);
                xy[q] = u32x2{pack2(bflo(xx[0]) * bflo(y[0]) * g, bfhi(xx[0]) * bfhi(y[0]) * g), pack2(bflo(xx[1]) * bflo(y[1]) * g, bfhi(xx[1]) * bfhi(y[1]) * g)};
            }
        }
        float g1 = 1.f - g;
        asm volatile("" : "+v"(xy[0]), "+v"(xy[1]), "+v"(xy[2]), "+v"(xy[3]), "+v"(g1));
        STAMP(3);
        barrier();
        {
            const u32x4 slow4 = lds16(sm + K2_CNT + 4 * C_SLOW);
            if (__builtin_amdgcn_readfirstlane((int)(slow4[0] | slow4[1] | slow4[2] | slow4[3])) != 0) barrier();
        }
        barrier();
        barrier();
        f32x16 acc = {};
#pragma unroll
        for (int i = 0; i < 8; ++i) acc = mfma32(wf[i], lds16(sm + K2_OI + r * 1024 + (((2 * i + h) ^ (r & 15)) << 4)), acc);
        const unsigned ob = lds0 + K2_OI + r * 1024 + ((h ^ (r & 15)) << 4);
        STAMP(4);
        barrier();
        STAMP(5);
        oproj12<8>(acc, wf + 8, ob);
        oproj12<20>(acc, wf + 20, ob);
        STAMP(6);
        u32x2 p[4];
#pragma unroll
        for (int q = 0; q < 4; ++q) {
            p[q][0] = pack2(fmaf(g1, acc[4 * q], bflo(xy[q][0])), fmaf(g1, acc[4 * q + 1], bfhi(xy[q][0])));
            p[q][1] = pack2(fmaf(g1, acc[4 * q + 2], bflo(xy[q][1])), fmaf(g1, acc[4 * q + 3], bfhi(xy[q][1])));
        }
        u32x4 b0, b1;
        quads_to_rows(p, b0, b1);
        char* op = (char*)out + (size_t)(tb * 32 + r) * 1024 + nt * 64 + h * 16;
        *(u32x4*)op = b0;
        *(u32x4*)(op + 32) = b1;
        STAMP(7);
    }
    STAMP_FLUSH(1);
}

}

extern "C" void kernel_launch(void* const* d_in, const int* in_sizes, int n_in, void* d_out, int out_size, void* d_ws, size_t ws_size,
                              hipStream_t stream) {
    const u16* A_q = (const u16*)d_in[0];
    const u16* A_k = (const u16*)d_in[1];
    const u16* A_v = (const u16*)d_in[2];
    const u16* A_shift = (const u16*)d_in[3];
    const u16* A_ssmk = (const u16*)d_in[4];
    const u16* A_ssmD = (const u16*)d_in[5];
    const u16* wq = (const u16*)d_in[7];
    const u16* wk = (const u16*)d_in[8];
    const u16* wv = (const u16*)d_in[9];
    const u16* wo = (const u16*)d_in[10];
    const u16* gw = (const u16*)d_in[11];
    char* ws = (char*)d_ws;
    k1_proj<<<256, 512, 0, stream>>>(A_q, A_k, A_v, A_shift, wq, wk, wv, wo, gw, ws);
    k2_main<<<256, 768, 0, stream>>>(A_q, A_ssmk, A_ssmD, ws, (u16*)d_out);
}
```

```cpp
#include <hip/hip_runtime.h>
#include <hip/hip_bf16.h>
#include <stdint.h>

typedef unsigned short u16;

namespace {

typedef __attribute__((ext_vector_type(8))) __bf16 bf16x8;
typedef __attribute__((ext_vector_type(16))) __bf16 bf16x16;
typedef __attribute__((ext_vector_type(2))) __bf16 bf16x2;
typedef __attribute__((ext_vector_type(4))) short s16x4;
typedef __attribute__((ext_vector_type(2))) float f32x2;
typedef __attribute__((ext_vector_type(4))) float f32x4;
typedef __attribute__((ext_vector_type(8))) float f32x8;
typedef __attribute__((ext_vector_type(16))) float f32x16;
typedef __attribute__((ext_vector_type(2))) unsigned u32x2;
typedef __attribute__((ext_vector_type(4))) unsigned u32x4;
typedef __amdgpu_buffer_rsrc_t rsrc_t;
#define LDSP __attribute__((address_space(3)))

constexpr int S_LEN = 2048, DM = 512;

constexpr unsigned WS_Q  = 0;
constexpr unsigned WS_UR = 2u << 20;
constexpr unsigned WS_O  = 4u << 20;
constexpr unsigned WS_YT = 6u << 20;
constexpr unsigned WS_KV = 8u << 20;
constexpr unsigned KF_BYTES = 65536, VF_BYTES = 64 * 1088, KV_BYTES = KF_BYTES + VF_BYTES;
constexpr unsigned WS_KN = WS_KV + KV_BYTES;
constexpr unsigned WS_G  = WS_KN + 1024;
#define NREPL 8
constexpr unsigned NREP = NREPL;
constexpr unsigned WS_F1 = WS_G + 8192;
constexpr unsigned WS_FY = WS_F1 + NREP * 2048;
constexpr unsigned WS_EP = WS_FY + 2048;
constexpr unsigned WS_WO = 9u << 20;
constexpr unsigned WS_END = WS_WO + (512u << 10);
constexpr unsigned POISON = 0xffffffffu;

#define STAMP_DECL
#define STAMP(i)
#define STAMP_FLUSH()
constexpr float QSCALE = 0.25f * 1.4426950408889634f;

__device__ __forceinline__ rsrc_t mk_rsrc(const void* p, unsigned bytes) {
    return __builtin_amdgcn_make_buffer_rsrc((void*)p, (short)0, (int)bytes, 0x00020000);
}
__device__ __forceinline__ void dma16(rsrc_t rs, unsigned voff, unsigned soff, unsigned ldsaddr) {
    asm volatile("s_nop 4\n\ts_mov_b32 m0, %0\n\ts_nop 0\n\tbuffer_load_dwordx4 %1, %2, %3 offen lds"
                 :: "s"(ldsaddr), "v"(voff), "s"(rs), "s"(soff) : "memory");
}
__device__ __forceinline__ void bload16(u32x4& dst, rsrc_t rs, unsigned voff) {
    asm volatile("buffer_load_dwordx4 %0, %1, %2, 0 offen" : "=v"(dst) : "v"(voff), "s"(rs) : "memory");
}
__device__ __forceinline__ void bload16_sc1(u32x4& dst, rsrc_t rs, unsigned voff) {
    asm volatile("buffer_load_dwordx4 %0, %1, %2, 0 offen sc1" : "=v"(dst) : "v"(voff), "s"(rs) : "memory");
}
__device__ __forceinline__ void bload8_sc1(u32x2& dst, rsrc_t rs, unsigned voff) {
    asm volatile("buffer_load_dwordx2 %0, %1, %2, 0 offen sc1" : "=v"(dst) : "v"(voff), "s"(rs) : "memory");
}
__device__ __forceinline__ void bload4(unsigned& dst, rsrc_t rs, unsigned voff) {
    asm volatile("buffer_load_dword %0, %1, %2, 0 offen" : "=v"(dst) : "v"(voff), "s"(rs) : "memory");
}
__device__ __forceinline__ void st16_sc1(rsrc_t rs, unsigned voff, u32x4 v) { __builtin_amdgcn_raw_buffer_store_b128(v, rs, voff, 0, 16); }
__device__ __forceinline__ void st4_sc1(rsrc_t rs, unsigned voff, unsigned v) { __builtin_amdgcn_raw_buffer_store_b32(v, rs, voff, 0, 16); }
__device__ __forceinline__ void st8_sc1(rsrc_t rs, unsigned voff, u32x2 v) { __builtin_amdgcn_raw_buffer_store_b64(v, rs, voff, 0, 16); }
__device__ __forceinline__ u32x4 ld16_sc1(rsrc_t rs, unsigned voff) { return __builtin_amdgcn_raw_buffer_load_b128(rs, voff, 0, 16); }
template <int N> __device__ __forceinline__ void wait_vm() { asm volatile("s_waitcnt vmcnt(%0)" :: "i"(N) : "memory"); }
__device__ __forceinline__ void wait_lgkm0() { asm volatile("s_waitcnt lgkmcnt(0)" ::: "memory"); }
__device__ __forceinline__ void barrier() { asm volatile("" ::: "memory"); __builtin_amdgcn_s_barrier(); asm volatile("" ::: "memory"); }

__device__ __forceinline__ unsigned pack2(float a, float b) {
    f32x2 v = {a, b};
    return __builtin_bit_cast(unsigned, __builtin_convertvector(v, bf16x2));
}
__device__ __forceinline__ float u2f(unsigned u) { return __builtin_bit_cast(float, u); }
__device__ __forceinline__ float bflo(unsigned u) { return __builtin_bit_cast(float, u << 16); }
__device__ __forceinline__ float bfhi(unsigned u) { return __builtin_bit_cast(float, u & 0xffff0000u); }
__device__ __forceinline__ float rbf(float a) { return bflo(pack2(a, 0.f)); }

__device__ __forceinline__ u32x4 lds16(const char LDSP* p) { return *(const u32x4 LDSP*)p; }
__device__ __forceinline__ u32x2 ldstr(const char LDSP* p) {
    return __builtin_bit_cast(u32x2, __builtin_amdgcn_ds_read_tr16_b64_v4i16((s16x4 LDSP*)p));
}
__device__ __forceinline__ bf16x8 asbf(u32x4 v) { return __builtin_bit_cast(bf16x8, v); }
__device__ __forceinline__ u32x4 cat2(u32x2 a, u32x2 b) { return u32x4{a[0], a[1], b[0], b[1]}; }
__device__ __forceinline__ f32x16 mfma32(u32x4 a, u32x4 b, f32x16 c) { return __builtin_amdgcn_mfma_f32_32x32x16_bf16(asbf(a), asbf(b), c, 0, 0, 0); }
__device__ __forceinline__ f32x4 mfma16(u32x4 a, u32x4 b, f32x4 c) { return __builtin_amdgcn_mfma_f32_16x16x32_bf16(asbf(a), asbf(b), c, 0, 0, 0); }

constexpr unsigned G_XS = 0, G_WS = 65536;

struct GemmAddr { unsigned xa[4]; unsigned wa; };
__device__ __forceinline__ GemmAddr gemm_addr(int wm, int wn, int lane) {
    GemmAddr g;
    const unsigned row = 32 * wm + (lane & 31), h = lane >> 5;
#pragma unroll
    for (int s = 0; s < 4; ++s) g.xa[s] = G_XS + row * 128 + (((2 * s + h) ^ ((row >> 1) & 7)) << 4);
    const unsigned G = lane >> 4, q = (lane & 15) >> 2, p = lane & 3;
    const unsigned c = 4 * wn + 2 * (G & 1) + (p >> 1);
    g.wa = G_WS + (8 * (G >> 1) + q) * 128 + ((c ^ (((q >> 1) & 1) << 2)) << 4) + 8 * (p & 1);
    return g;
}
__device__ __forceinline__ void gemm_chunk(const char LDSP* sm, const GemmAddr& g, int kc, f32x16& acc) {
    u32x4 xb[4]; u32x2 w0[4], w1[4];
#pragma unroll
    for (int s = 0; s < 4; ++s) {
        xb[s] = lds16(sm + g.xa[s] + kc * 8192);
        w0[s] = ldstr(sm + g.wa + kc * 8192 + s * 2048);
        w1[s] = ldstr(sm + g.wa + kc * 8192 + s * 2048 + 512);
    }
#pragma unroll
    for (int s = 0; s < 4; ++s) acc = mfma32(cat2(w0[s], w1[s]), xb[s], acc);
}
__device__ __forceinline__ void gemm_stage(const char LDSP* sm, const GemmAddr& g, int kc0, f32x16& acc) {
    u32x4 xb[8]; u32x2 w0[8], w1[8];
#pragma unroll
    for (int s = 0; s < 8; ++s) {
        const int kc = kc0 + (s >> 2), ss = s & 3;
        xb[s] = lds16(sm + g.xa[ss] + kc * 8192);
        w0[s] = ldstr(sm + g.wa + kc * 8192 + ss * 2048);
        w1[s] = ldstr(sm + g.wa + kc * 8192 + ss * 2048 + 512);
    }
#pragma unroll
    for (int s = 0; s < 8; ++s) acc = mfma32(cat2(w0[s], w1[s]), xb[s], acc);
}
__device__ __forceinline__ void quads_to_rows(u32x2 (&p)[4], u32x4& r0, u32x4& r1) {
    auto s0 = __builtin_amdgcn_permlane32_swap(p[0][0], p[1][0], false, false);
    auto s1 = __builtin_amdgcn_permlane32_swap(p[0][1], p[1][1], false, false);
    auto s2 = __builtin_amdgcn_permlane32_swap(p[2][0], p[3][0], false, false);
    auto s3 = __builtin_amdgcn_permlane32_swap(p[2][1], p[3][1], false, false);
    r0 = u32x4{s0[0], s1[0], s0[1], s1[1]};
    r1 = u32x4{s2[0], s3[0], s2[1], s3[1]};
}
__device__ __forceinline__ unsigned wave_umax(unsigned v) {
    v = max(v, (unsigned)__builtin_amdgcn_update_dpp(0, (int)v, 0x111, 0xf, 0xf, true));
    v = max(v, (unsigned)__builtin_amdgcn_update_dpp(0, (int)v, 0x112, 0xf, 0xf, true));
    v = max(v, (unsigned)__builtin_amdgcn_update_dpp(0, (int)v, 0x114, 0xf, 0xf, true));
    v = max(v, (unsigned)__builtin_amdgcn_update_dpp(0, (int)v, 0x118, 0xf, 0xf, true));
    v = max(v, (unsigned)__builtin_amdgcn_update_dpp(0, (int)v, 0x142, 0xa, 0xf, false));
    v = max(v, (unsigned)__builtin_amdgcn_update_dpp(0, (int)v, 0x143, 0xc, 0xf, false));
    return (unsigned)__builtin_amdgcn_readlane((int)v, 63);
}

__device__ __forceinline__ unsigned lds_ld(const char LDSP* p) { return *(const volatile unsigned LDSP*)p; }
__device__ __forceinline__ void lds_wait(const char LDSP* p, unsigned n) {
    while ((unsigned)__builtin_amdgcn_readfirstlane((int)lds_ld(p)) < n) __builtin_amdgcn_s_sleep(1);
    asm volatile("" ::: "memory");
}
__device__ __forceinline__ void lds_post(char LDSP* p, int lane) {
    asm volatile("" ::: "memory");
    if (lane == 0) __hip_atomic_fetch_add((unsigned LDSP*)p, 1u, __ATOMIC_RELAXED, __HIP_MEMORY_SCOPE_WORKGROUP);
    asm volatile("" ::: "memory");
}
__device__ __forceinline__ unsigned lds_inc(char LDSP* p, int lane) {
    unsigned old = 0;
    asm volatile("" ::: "memory");
    if (lane == 0) old = __hip_atomic_fetch_add((unsigned LDSP*)p, 1u, __ATOMIC_RELAXED, __HIP_MEMORY_SCOPE_WORKGROUP);
    old = (unsigned)__builtin_amdgcn_readfirstlane((int)old);
    asm volatile("" ::: "memory");
    return old;
}
__device__ __forceinline__ void dma1(rsrc_t rs, unsigned voff, unsigned soff, unsigned ldsaddr) {
    asm volatile("s_mov_b32 m0, %0\n\ts_nop 0\n\tbuffer_load_dwordx4 %1, %2, %3 offen lds"
                 :: "s"(ldsaddr), "v"(voff), "s"(rs), "s"(soff) : "memory");
}
__device__ __forceinline__ void dma2(rsrc_t rs, unsigned voff, unsigned soff, unsigned ldsaddr) {
    asm volatile("s_mov_b32 m0, %0\n\ts_nop 0\n\tbuffer_load_dwordx4 %1, %2, %3 offen lds\n\tbuffer_load_dwordx4 %1, %2, %3 offen offset:1024 lds"
                 :: "s"(ldsaddr), "v"(voff), "s"(rs), "s"(soff) : "memory");
}
__device__ __forceinline__ void dma4(rsrc_t rs, unsigned voff, unsigned soff, unsigned ldsaddr) {
    asm volatile("s_mov_b32 m0, %0\n\ts_nop 0\n\tbuffer_load_dwordx4 %1, %2, %3 offen lds\n\tbuffer_load_dwordx4 %1, %2, %3 offen offset:1024 lds\n\t"
                 "buffer_load_dwordx4 %1, %2, %3 offen offset:2048 lds\n\tbuffer_load_dwordx4 %1, %2, %3 offen offset:3072 lds"
                 :: "s"(ldsaddr), "v"(voff), "s"(rs), "s"(soff) : "memory");
}
__device__ __forceinline__ void dma2_sc1(rsrc_t rs, unsigned voff, unsigned soff, unsigned ldsaddr) {
    asm volatile("s_mov_b32 m0, %0\n\ts_nop 0\n\tbuffer_load_dwordx4 %1, %2, %3 offen sc1 lds\n\tbuffer_load_dwordx4 %1, %2, %3 offen offset:1024 sc1 lds"
                 :: "s"(ldsaddr), "v"(voff), "s"(rs), "s"(soff) : "memory");
}
__device__ __forceinline__ void dma4_sc1(rsrc_t rs, unsigned voff, unsigned soff, unsigned ldsaddr) {
    asm volatile("s_mov_b32 m0, %0\n\ts_nop 0\n\tbuffer_load_dwordx4 %1, %2, %3 offen sc1 lds\n\tbuffer_load_dwordx4 %1, %2, %3 offen offset:1024 sc1 lds\n\t"
                 "buffer_load_dwordx4 %1, %2, %3 offen offset:2048 sc1 lds\n\tbuffer_load_dwordx4 %1, %2, %3 offen offset:3072 sc1 lds"
                 :: "s"(ldsaddr), "v"(voff), "s"(rs), "s"(soff) : "memory");
}
__device__ __forceinline__ float dot2(unsigned a, unsigned b, float c) {
    return __builtin_amdgcn_fdot2_f32_bf16(__builtin_bit_cast(bf16x2, a), __builtin_bit_cast(bf16x2, b), c, false);
}

constexpr unsigned K1_WE = 131072;
constexpr unsigned K1_KT = K1_WE + 16384;
constexpr unsigned K1_WO = K1_KT + 8192;
constexpr unsigned K2_K = 0, K2_V = 65536, K2_H = KV_BYTES, K2_HC = 4160, K2_U = K2_H + 4 * K2_HC, K2_UC = 4608;
constexpr unsigned K2_CNT = K2_U + 2 * K2_UC;
constexpr unsigned K2_LDS = K2_CNT + 128;
constexpr unsigned K2_XP = K2_H, K2_YP = K2_H + 8192;
constexpr unsigned K2_PART = 0;
constexpr unsigned K2_OI = 32768;
#define XSLEEP 10
enum { C_ST0 = 0, C_ST1 = 1, C_ST2 = 2, C_ST3 = 3, C_CH0 = 4, C_CH1 = 5, C_YST = 6, C_GO = 7, C_SLOW = 8  ,
       D_S0 = 12, D_S1 = 13, D_S2 = 14, D_S3 = 15, D_ALL = 16, D_TS = 17, D_P1 = 18, D_TSF = 19, D_QS = 20 };
static_assert(K1_WO + 2048 <= K2_CNT, "phase 1 images overlap the counters");

#define AE(X, i) "v_exp_f32 v[" #X "+" #i "], v[" #X "+" #i "]\n\t"
#define ASTEP(X0, X1, Y0, Y1, t)                                                             \
    AE(X0, 0) AE(X0, 1) AE(X0, 2) AE(X0, 3)                                                  \
    "s_waitcnt lgkmcnt(2)\n\t"                                                               \
    "v_mfma_f32_32x32x16_bf16 v[" #Y0 ":" #Y0 "+15], %2, %10, 0\n\t"                         \
    AE(X0, 4) AE(X0, 5) AE(X0, 6) AE(X0, 7)                                                  \
    "v_mfma_f32_32x32x16_bf16 v[" #Y1 ":" #Y1 "+15], %2, %11, 0\n\t"                         \
    "ds_read_b128 %2, %5 offset:(" #t "+2)*1024\n\t"                                         \
    AE(X0, 8) AE(X0, 9) AE(X0, 10) AE(X0, 11)                                                \
    "s_waitcnt lgkmcnt(2)\n\t"                                                               \
    "v_smfmac_f32_32x32x32_bf16 %0, %3, v[" #X0 ":" #X0 "+7], %12\n\t"                       \
    AE(X0, 12) AE(X0, 13) AE(X0, 14) AE(X0, 15) AE(X1, 0) AE(X1, 1)                          \
    "s_waitcnt lgkmcnt(1)\n\t"                                                               \
    "v_smfmac_f32_32x32x32_bf16 %0, %4, v[" #X0 "+8:" #X0 "+15], %12\n\t"                    \
    AE(X1, 2) AE(X1, 3) AE(X1, 4) AE(X1, 5) AE(X1, 6) AE(X1, 7) AE(X1, 8) AE(X1, 9)          \
    "v_smfmac_f32_32x32x32_bf16 %1, %3, v[" #X1 ":" #X1 "+7], %12\n\t"                       \
    "ds_read_b128 %3, %6 offset:(" #t "+1)*1088\n\t"                                         \
    AE(X1, 10) AE(X1, 11) AE(X1, 12) AE(X1, 13) AE(X1, 14) AE(X1, 15)                        \
    "s_nop 1\n\t"                                                                            \
    "v_smfmac_f32_32x32x32_bf16 %1, %4, v[" #X1 "+8:" #X1 "+15], %12\n\t"                    \
    "ds_read_b128 %4, %6 offset:(" #t "+1)*1088+544\n\t"
#define ASTEP_E(t) ASTEP(0, 16, 32, 48, t)
#define ASTEP_O(t) ASTEP(32, 48, 0, 16, t)
#define AWAIT(lbl, off)                                                                      \
    #lbl ":\n\t"                                                                             \
    "ds_read_b32 %7, %13 offset:" #off "\n\t"                                                \
    "s_waitcnt lgkmcnt(0)\n\t"                                                               \
    "v_readfirstlane_b32 %8, %7\n\t"                                                         \
    "s_nop 3\n\t"                                                                            \
    "s_cmp_ge_u32 %8, 4\n\t"                                                                 \
    "s_cbranch_scc1 " #lbl "9f\n\t"                                                          \
    "s_sleep 1\n\t"                                                                          \
    "s_branch " #lbl "b\n\t"                                                                 \
    #lbl "9:\n\t"

__device__ __forceinline__ void attn_fast(f32x16& O0, f32x16& O1, u32x4 q0, u32x4 q1, unsigned kaddr, unsigned vaddr, unsigned caddr) {
    u32x4 kf, v0, v1;
    unsigned idx = 0xddddu, tv; int ts, it = 0;
    asm volatile(
        "s_setprio 2\n\t"
        AWAIT(1, 0)
        "ds_read_b128 %2, %5\n\t"
        "s_waitcnt lgkmcnt(0)\n\t"
        "v_mfma_f32_32x32x16_bf16 v[0:15], %2, %10, 0\n\t"
        "v_mfma_f32_32x32x16_bf16 v[16:31], %2, %11, 0\n\t"
        "ds_read_b128 %2, %5 offset:1024\n\t"
        "ds_read_b128 %3, %6\n\t"
        "ds_read_b128 %4, %6 offset:544\n\t"
        "s_nop 10\n\t"
        "4:\n\t"
        ASTEP_E(0) ASTEP_O(1)
        "s_cmp_gt_u32 %9, 1\n\t"
        "s_cbranch_scc1 5f\n\t"
        "s_cmp_eq_u32 %9, 0\n\t"
        "s_cbranch_scc0 7f\n\t"
        AWAIT(2, 4)
        "s_branch 5f\n\t"
        "7:\n\t"
        AWAIT(8, 12)
        "5:\n\t"
        ASTEP_E(2) ASTEP_O(3) ASTEP_E(4) ASTEP_O(5)
        "s_cmp_lg_u32 %9, 0\n\t"
        "s_cbranch_scc1 6f\n\t"
        AWAIT(3, 8)
        "6:\n\t"
        ASTEP_E(6) ASTEP_O(7)
        "v_add_u32 %5, 0x2000, %5\n\t"
        "v_add_u32 %6, 0x2200, %6\n\t"
        "s_add_u32 %9, %9, 1\n\t"
        "s_cmp_eq_u32 %9, 2\n\t"
        "s_cbranch_scc0 10f\n\t"
        "s_setprio 1\n\t"
        "10:\n\t"
        "s_cmp_eq_u32 %9, 3\n\t"
        "s_cbranch_scc0 11f\n\t"
        "s_setprio 0\n\t"
        "11:\n\t"
        "s_cmp_lg_u32 %9, 4\n\t"
        "s_cbranch_scc1 4b\n\t"
        "s_waitcnt lgkmcnt(0)\n\t"
        "s_nop 15\n\t"
        "s_nop 7\n\t"
        : "+v"(O0), "+v"(O1), "=&v"(kf), "=&v"(v0), "=&v"(v1), "+v"(kaddr), "+v"(vaddr), "=&v"(tv), "=&s"(ts), "+s"(it)
        : "v"(q0), "v"(q1), "v"(idx), "v"(caddr)
        : "v0", "v1", "v2", "v3", "v4", "v5", "v6", "v7", "v8", "v9", "v10", "v11", "v12", "v13", "v14", "v15",
          "v16", "v17", "v18", "v19", "v20", "v21", "v22", "v23", "v24", "v25", "v26", "v27", "v28", "v29", "v30", "v31",
          "v32", "v33", "v34", "v35", "v36", "v37", "v38", "v39", "v40", "v41", "v42", "v43", "v44", "v45", "v46", "v47",
          "v48", "v49", "v50", "v51", "v52", "v53", "v54", "v55", "v56", "v57", "v58", "v59", "v60", "v61", "v62", "v63",
          "scc", "memory");
}

__device__ __forceinline__ void attn_slow(f32x16& O, float& m, u32x4 q, const char LDSP* sm, unsigned kaddr, unsigned vaddr) {
#pragma unroll 1
    for (int t = 0; t < 32; ++t) {
        const u32x4 kf = lds16(sm + kaddr + t * 1024);
        const u32x4 v0 = lds16(sm + vaddr + t * 1088), v1 = lds16(sm + vaddr + t * 1088 + 544);
        f32x16 s = mfma32(kf, q, f32x16{});
        float tm = s[0];
#pragma unroll
        for (int r = 1; r < 16; ++r) tm = fmaxf(tm, s[r]);
        tm = fmaxf(tm, __shfl_xor(tm, 32));
        const float mn = fmaxf(m, tm);
        const float alpha = __builtin_amdgcn_exp2f(m - mn);
        m = mn;
#pragma unroll
        for (int r = 0; r < 16; ++r) { O[r] *= alpha; s[r] = __builtin_amdgcn_exp2f(s[r] - mn); }
        const f32x8 slo = __builtin_shufflevector(s, s, 0, 1, 2, 3, 4, 5, 6, 7), shi = __builtin_shufflevector(s, s, 8, 9, 10, 11, 12, 13, 14, 15);
        O = __builtin_amdgcn_smfmac_f32_32x32x32_bf16(asbf(v0), __builtin_bit_cast(bf16x16, slo), O, 0xdddd, 0, 0);
        O = __builtin_amdgcn_smfmac_f32_32x32x32_bf16(asbf(v1), __builtin_bit_cast(bf16x16, shi), O, 0xdddd, 0, 0);
    }
}

template <int SET>
__device__ __forceinline__ void conv_wave(const char LDSP* sm, int ch, int lane, float dskip, rsrc_t rsWS, unsigned yoff) {
    constexpr int T0 = SET ? 1 : 0, T1 = SET ? 2 : 3, T2 = SET ? 5 : 4, T3 = SET ? 6 : 7;
    constexpr int NP = T3 + 1;
    const int b = lane & 15, g = lane >> 4;
    const int c = (b + 1) & 1;
    unsigned ha[4];
#pragma unroll
    for (int i = 0; i < 4; ++i) {
        ha[i] = K2_H + (ch * 2 + c) * K2_HC + ((b + 1 - c) / 2 + 4 * g) * 4 + i * 1024;
        asm volatile("" : "+v"(ha[i]));
    }
    unsigned ua = K2_U + ch * K2_UC + (2032 - 16 * b + 8 * g) * 2 - 7 * 512;
    asm volatile("" : "+v"(ua));
    f32x4 acc[4] = {};
#pragma unroll
    for (int r = 0; r < 8; ++r) {
        u32x4 A[NP], B[NP];
#pragma unroll
        for (int p = 0; p < NP; ++p) {
            const unsigned LDSP* hp = (const unsigned LDSP*)(sm + ha[p >> 1] + r * 64 + (p & 1) * 512);
            A[p] = u32x4{hp[0], hp[1], hp[2], hp[3]};
            B[p] = lds16(sm + ua + r * 64 + (7 - p) * 512);
        }
#pragma unroll
        for (int p = 0; p < NP; ++p) {
            if (p <= T0) acc[0] = mfma16(A[p], B[T0 - p < 0 ? 0 : T0 - p], acc[0]);
            if (p <= T1) acc[1] = mfma16(A[p], B[T1 - p < 0 ? 0 : T1 - p], acc[1]);
            if (p <= T2) acc[2] = mfma16(A[p], B[T2 - p < 0 ? 0 : T2 - p], acc[2]);
            if (p <= T3) acc[3] = mfma16(A[p], B[T3 - p], acc[3]);
        }
    }
    constexpr int TI[4] = {T0, T1, T2, T3};
#pragma unroll
    for (int i = 0; i < 4; ++i) {
        const int s = 256 * TI[i] + 16 * b + 4 * g;
        const u32x2 u = *(const u32x2 LDSP*)(sm + K2_U + ch * K2_UC + (2044 - s) * 2);
        const float y0 = rbf(acc[i][0]) + rbf(bfhi(u[1]) * dskip), y1 = rbf(acc[i][1]) + rbf(bflo(u[1]) * dskip);
        const float y2 = rbf(acc[i][2]) + rbf(bfhi(u[0]) * dskip), y3 = rbf(acc[i][3]) + rbf(bflo(u[0]) * dskip);
        st8_sc1(rsWS, yoff + s * 2, u32x2{pack2(y0, y1), pack2(y2, y3)});
    }
}

template <int I0>
__device__ __forceinline__ void oproj12(f32x16& acc, const u32x4* wf, unsigned ob) {
    u32x4 t0, t1, t2, t3; unsigned a;
    asm volatile(
        "v_xor_b32 %5, (((%c19+0)&7)<<5), %18\n\t" "ds_read_b128 %1, %5 offset:((%c19+0)>>3)*256\n\t"
        "v_xor_b32 %5, (((%c19+1)&7)<<5), %18\n\t" "ds_read_b128 %2, %5 offset:((%c19+1)>>3)*256\n\t"
        "v_xor_b32 %5, (((%c19+2)&7)<<5), %18\n\t" "ds_read_b128 %3, %5 offset:((%c19+2)>>3)*256\n\t"
        "v_xor_b32 %5, (((%c19+3)&7)<<5), %18\n\t" "ds_read_b128 %4, %5 offset:((%c19+3)>>3)*256\n\t"
        "s_waitcnt lgkmcnt(3)\n\t" "v_mfma_f32_32x32x16_bf16 %0, %6, %1, %0\n\t"
        "v_xor_b32 %5, (((%c19+4)&7)<<5), %18\n\t" "ds_read_b128 %1, %5 offset:((%c19+4)>>3)*256\n\t"
        "s_waitcnt lgkmcnt(3)\n\t" "v_mfma_f32_32x32x16_bf16 %0, %7, %2, %0\n\t"
        "v_xor_b32 %5, (((%c19+5)&7)<<5), %18\n\t" "ds_read_b128 %2, %5 offset:((%c19+5)>>3)*256\n\t"
        "s_waitcnt lgkmcnt(3)\n\t" "v_mfma_f32_32x32x16_bf16 %0, %8, %3, %0\n\t"
        "v_xor_b32 %5, (((%c19+6)&7)<<5), %18\n\t" "ds_read_b128 %3, %5 offset:((%c19+6)>>3)*256\n\t"
        "s_waitcnt lgkmcnt(3)\n\t" "v_mfma_f32_32x32x16_bf16 %0, %9, %4, %0\n\t"
        "v_xor_b32 %5, (((%c19+7)&7)<<5), %18\n\t" "ds_read_b128 %4, %5 offset:((%c19+7)>>3)*256\n\t"
        "s_waitcnt lgkmcnt(3)\n\t" "v_mfma_f32_32x32x16_bf16 %0, %10, %1, %0\n\t"
        "v_xor_b32 %5, (((%c19+8)&7)<<5), %18\n\t" "ds_read_b128 %1, %5 offset:((%c19+8)>>3)*256\n\t"
        "s_waitcnt lgkmcnt(3)\n\t" "v_mfma_f32_32x32x16_bf16 %0, %11, %2, %0\n\t"
        "v_xor_b32 %5, (((%c19+9)&7)<<5), %18\n\t" "ds_read_b128 %2, %5 offset:((%c19+9)>>3)*256\n\t"
        "s_waitcnt lgkmcnt(3)\n\t" "v_mfma_f32_32x32x16_bf16 %0, %12, %3, %0\n\t"
        "v_xor_b32 %5, (((%c19+10)&7)<<5), %18\n\t" "ds_read_b128 %3, %5 offset:((%c19+10)>>3)*256\n\t"
        "s_waitcnt lgkmcnt(3)\n\t" "v_mfma_f32_32x32x16_bf16 %0, %13, %4, %0\n\t"
        "v_xor_b32 %5, (((%c19+11)&7)<<5), %18\n\t" "ds_read_b128 %4, %5 offset:((%c19+11)>>3)*256\n\t"
        "s_waitcnt lgkmcnt(3)\n\t" "v_mfma_f32_32x32x16_bf16 %0, %14, %1, %0\n\t"
        "s_waitcnt lgkmcnt(2)\n\t" "v_mfma_f32_32x32x16_bf16 %0, %15, %2, %0\n\t"
        "s_waitcnt lgkmcnt(1)\n\t" "v_mfma_f32_32x32x16_bf16 %0, %16, %3, %0\n\t"
        "s_waitcnt lgkmcnt(0)\n\t" "v_mfma_f32_32x32x16_bf16 %0, %17, %4, %0\n\t"
        "s_nop 15\n\t"
        : "+v"(acc), "=&v"(t0), "=&v"(t1), "=&v"(t2), "=&v"(t3), "=&v"(a)
        : "v"(wf[0]), "v"(wf[1]), "v"(wf[2]), "v"(wf[3]), "v"(wf[4]), "v"(wf[5]), "v"(wf[6]), "v"(wf[7]), "v"(wf[8]), "v"(wf[9]), "v"(wf[10]), "v"(wf[11]),
          "v"(ob), "i"(I0)
        : "memory");
}

}

#define KVSLEEP 8
#define POLLSLEEP 4

extern "C" __global__ __launch_bounds__(768) void gm_fused(const u16* __restrict__ x, const u16* __restrict__ kin, const u16* __restrict__ vin,
                                                           const u16* __restrict__ shiftk, const u16* __restrict__ ssmk, const u16* __restrict__ ssmD,
                                                           const u16* __restrict__ wq, const u16* __restrict__ wk, const u16* __restrict__ wv,
                                                           const u16* __restrict__ wo, const u16* __restrict__ gw, char* __restrict__ ws,
                                                           u16* __restrict__ out) {
    __shared__ __attribute__((aligned(16))) char smem[K2_LDS];
    constexpr unsigned CNT = K2_CNT;
    const char LDSP* sm = (const char LDSP*)smem;
    char LDSP* smw = (char LDSP*)smem;
    const unsigned lds0 = (unsigned)(size_t)sm;
    const int lane = threadIdx.x & 63;
    const int w = __builtin_amdgcn_readfirstlane(threadIdx.x >> 6);
    const int bid = blockIdx.x;
    asm volatile("" :: "s"(x), "s"(kin), "s"(vin), "s"(shiftk), "s"(ssmk), "s"(ssmD), "s"(wq), "s"(wk), "s"(wv), "s"(wo), "s"(gw), "s"(ws), "s"(out));
    const rsrc_t rsWS = mk_rsrc(ws, WS_END);
    const int tt = (bid & 7) * 4 + (bid >> 6), j = (bid >> 3) & 7;
    const int tb = (bid & 7) * 8 + (bid >> 5), hg = (bid >> 3) & 3;
    const int r = lane & 31, h = lane >> 5;
    STAMP_DECL;
    STAMP(0);
    u32x2 ep;
    bload8_sc1(ep, rsWS, WS_EP);
    if (w == 0) { if (lane < 32) *(unsigned LDSP*)(smw + CNT + lane * 4) = 0; wait_lgkm0(); }
    barrier();

    if (w >= 8) {
        const int lw = w - 8;
        __builtin_amdgcn_s_setprio(3);
        const rsrc_t rsX = mk_rsrc(x, S_LEN * DM * 2), rsW = mk_rsrc(wq, DM * DM * 2);
        const unsigned gr = lane >> 3, gc = lane & 7;
        const unsigned row0 = 16 * lw + gr;
        const unsigned vx0 = (tt * 64 + row0) * 1024 + ((gc ^ ((row0 >> 1) & 7)) << 4);
        const unsigned vx1 = (tt * 64 + row0 + 8) * 1024 + ((gc ^ (((row0 + 8) >> 1) & 7)) << 4);
        const unsigned vw0 = row0 * 1024 + j * 128 + ((gc ^ (((gr >> 1) & 1) << 2)) << 4);
        auto chunk = [&](int kc) {
            dma1(rsX, vx0, kc * 128, lds0 + G_XS + kc * 8192 + lw * 2048);
            dma1(rsX, vx1, kc * 128, lds0 + G_XS + kc * 8192 + lw * 2048 + 1024);
            dma1(rsW, vw0, kc * 65536, lds0 + G_WS + kc * 8192 + lw * 2048);
            dma1(rsW, vw0, kc * 65536 + 8192, lds0 + G_WS + kc * 8192 + lw * 2048 + 1024);
        };
        if (j < 2) {
            const rsrc_t rsE = mk_rsrc(j == 0 ? wk : wv, DM * 16 * 2);
#pragma unroll
            for (int i = 0; i < 4; ++i) {
                const unsigned prow = (lw * 4 + i) * 32 + (lane >> 1);
                const unsigned grow = prow ^ (((prow >> 3) & 1) << 2);
                dma1(rsE, grow * 32 + (lane & 1) * 16, 0, lds0 + K1_WE + (lw * 4 + i) * 1024);
            }
        } else if (j == 2) {
            if (lw == 0) dma1(mk_rsrc(gw, DM * 2), lane * 16, 0, lds0 + K1_WE);
        }
        if (lw >= 2)
            dma1(mk_rsrc(wo, DM * DM * 2), (32 * (bid & 15) + 16 * (lw - 2) + (lane >> 2)) * 1024 + (bid >> 4) * 64 + (lane & 3) * 16, 0,
                 lds0 + K1_WO + (lw - 2) * 1024);
        chunk(0); chunk(1); chunk(2); chunk(3);
        chunk(4); chunk(5);
        wait_vm<16>(); lds_post(smw + CNT + 4 * D_S0, lane);
        chunk(6); chunk(7);
        wait_vm<16>(); lds_post(smw + CNT + 4 * D_S1, lane);
        wait_vm<8>();  lds_post(smw + CNT + 4 * D_S2, lane);
        wait_vm<4>();  lds_post(smw + CNT + 4 * D_S3, lane);
        wait_vm<0>();  lds_post(smw + CNT + 4 * D_ALL, lane);
        STAMP(1);
        lds_wait(sm + CNT + 4 * D_ALL, 4);
        if (lw >= 2) {
            const unsigned G = lane >> 4, q = (lane & 15) >> 2, p = lane & 3;
            const unsigned a = K1_WO + (16 * (lw - 2) + 8 * (G >> 1) + q) * 64 + (16 * (G & 1) + 4 * p) * 2;
            st16_sc1(rsWS, WS_WO + ((bid >> 4) * 32 + 2 * (bid & 15) + (lw - 2)) * 1024 + lane * 16, cat2(ldstr(sm + a), ldstr(sm + a + 256)));
        }
        if (j < 3) {
            const unsigned row = 16 * lw + (lane & 15), g = lane >> 4;
            const unsigned ex0 = G_XS + row * 128 + ((g ^ ((row >> 1) & 7)) << 4), ex1 = G_XS + row * 128 + (((4 + g) ^ ((row >> 1) & 7)) << 4);
            const unsigned q = (lane & 15) >> 2, p = lane & 3, n = lane & 15;
            const unsigned ee0 = K1_WE + (8 * g + q + 4 * (0 ^ (g & 1))) * 32 + 8 * p, ee1 = K1_WE + (8 * g + q + 4 * (1 ^ (g & 1))) * 32 + 8 * p;
            f32x4 e0 = {}, e1 = {};
            if (j == 0) {
#pragma unroll
                for (int kc = 0; kc < 8; ++kc) {
                    e0 = mfma16(cat2(ldstr(sm + ee0 + kc * 2048), ldstr(sm + ee1 + kc * 2048)), lds16(sm + ex0 + kc * 8192), e0);
                    e1 = mfma16(cat2(ldstr(sm + ee0 + kc * 2048 + 1024), ldstr(sm + ee1 + kc * 2048 + 1024)), lds16(sm + ex1 + kc * 8192), e1);
                }
                const unsigned p0 = pack2(e0[0] + e1[0], e0[1] + e1[1]), p1 = pack2(e0[2] + e1[2], e0[3] + e1[3]);
                const int key = tt * 64 + 16 * lw + n;
                st8_sc1(rsWS, WS_KV + (key >> 5) * 1024 + (g >> 1) * 512 + (key & 31) * 16 + (g & 1) * 8, u32x2{p0, p1});
                float ss = dot2(p0, p0, dot2(p1, p1, 0.f));
                ss += __shfl_xor(ss, 16); ss += __shfl_xor(ss, 32);
                const unsigned m = wave_umax(__builtin_bit_cast(unsigned, ss));
                if (lane == 0) st4_sc1(rsWS, WS_KN + (tt * 4 + lw) * 4, m);
            } else if (j == 1) {
#pragma unroll
                for (int kc = 0; kc < 8; ++kc) {
                    e0 = mfma16(lds16(sm + ex0 + kc * 8192), cat2(ldstr(sm + ee0 + kc * 2048), ldstr(sm + ee1 + kc * 2048)), e0);
                    e1 = mfma16(lds16(sm + ex1 + kc * 8192), cat2(ldstr(sm + ee0 + kc * 2048 + 1024), ldstr(sm + ee1 + kc * 2048 + 1024)), e1);
                }
                const unsigned p0 = pack2(e0[0] + e1[0], e0[1] + e1[1]), p1 = pack2(e0[2] + e1[2], e0[3] + e1[3]);
                const int tile = 2 * tt + (lw >> 1), pp = lw & 1;
                const unsigned vb = WS_KV + KF_BYTES + tile * 1088 + pp * 544;
                st8_sc1(rsWS, vb + ((g >> 1) * 16 + n) * 16 + (g & 1) * 8, u32x2{p0, p1});
                if (lane < 2) {
                    const unsigned f = lane == 0 ? 0x3f803f80u : 0u;
                    st16_sc1(rsWS, vb + (32 + lane) * 16, u32x4{f, f, f, f});
                }
                const unsigned a0 = p0 & 0x7fff7fffu, a1 = p1 & 0x7fff7fffu;
                const unsigned m = wave_umax(max(max(a0 & 0xffffu, a0 >> 16), max(a1 & 0xffffu, a1 >> 16)));
                if (lane == 0) st4_sc1(rsWS, WS_KN + (128 + tt * 4 + lw) * 4, m << 16);
            } else {
#pragma unroll
                for (int kc = 0; kc < 8; ++kc) {
                    e0 = mfma16(lds16(sm + ex0 + kc * 8192), lds16(sm + K1_WE + g * 16 + kc * 128), e0);
                    e1 = mfma16(lds16(sm + ex1 + kc * 8192), lds16(sm + K1_WE + g * 16 + kc * 128 + 64), e1);
                }
                if (n == 0) {
                    u32x4 gv;
#pragma unroll
                    for (int r = 0; r < 4; ++r) gv[r] = __builtin_bit_cast(unsigned, rbf(1.f / (1.f + __expf(-rbf(e0[r] + e1[r])))));
                    st16_sc1(rsWS, WS_G + (tt * 64 + 16 * lw + 4 * g) * 4, gv);
                }
            }
        }
        asm volatile("s_waitcnt vmcnt(0)" : "+v"(ep) :: "memory");
        STAMP(3);
    } else if (w >= 4) {
        const int kw = w - 4;
        __builtin_amdgcn_s_sleep(KVSLEEP);
        const rsrc_t rsV = mk_rsrc(vin, S_LEN * DM * 2), rsS = mk_rsrc(shiftk, DM * 4 * 2), rsK = mk_rsrc(kin, S_LEN * DM * 2);
        const int oct = lane & 7, t0 = 16 * kw + 2 * (lane >> 3);
        u32x4 va, vb, sk0, sk1, sk2, sk3;
        u32x4 R0, R1, R2, R3, R4;
        {
            const unsigned vk = (unsigned)((tt * 64 - 3 + t0) * 1024 + j * 128 + oct * 16);
            bload16(R0, rsK, vk); bload16(R1, rsK, vk + 1024); bload16(R2, rsK, vk + 2048); bload16(R3, rsK, vk + 3072); bload16(R4, rsK, vk + 4096);
        }
        bload16(va, rsV, (tt * 64 + t0) * 1024 + j * 128 + oct * 16);
        bload16(vb, rsV, (tt * 64 + t0 + 1) * 1024 + j * 128 + oct * 16);
        {
            const unsigned so = (j * 64 + oct * 8) * 8;
            bload16(sk0, rsS, so); bload16(sk1, rsS, so + 16); bload16(sk2, rsS, so + 32); bload16(sk3, rsS, so + 48);
        }
        asm volatile("s_waitcnt vmcnt(0)" : "+v"(va), "+v"(vb), "+v"(sk0), "+v"(sk1), "+v"(sk2), "+v"(sk3), "+v"(R0), "+v"(R1), "+v"(R2), "+v"(R3), "+v"(R4),
                     "+v"(ep) :: "memory");
        STAMP(1);
#pragma unroll
        for (int i = 0; i < 2; ++i)
            st16_sc1(rsWS, WS_O + (tb * 32 + 8 * kw + 4 * i + (lane >> 4)) * 1024 + hg * 256 + (lane & 15) * 16, u32x4{POISON, POISON, POISON, POISON});
        {
            const u32x4 R[5] = {R0, R1, R2, R3, R4};
            const u32x4 sk[4] = {sk0, sk1, sk2, sk3};
            unsigned tw[8];
#pragma unroll
            for (int c = 0; c < 8; ++c) {
                const int d = c >> 1;
                const unsigned sel = (c & 1) ? 0x07060302u : 0x05040100u;
                const unsigned s01 = sk[d][(c & 1) * 2], s23 = sk[d][(c & 1) * 2 + 1];
                const float a0 = dot2(__builtin_amdgcn_perm(R[2][d], R[3][d], sel), s01, dot2(__builtin_amdgcn_perm(R[0][d], R[1][d], sel), s23, 0.f));
                const float a1 = dot2(__builtin_amdgcn_perm(R[3][d], R[4][d], sel), s01, dot2(__builtin_amdgcn_perm(R[1][d], R[2][d], sel), s23, 0.f));
                const float v0 = (c & 1) ? bfhi(va[d]) : bflo(va[d]), v1 = (c & 1) ? bfhi(vb[d]) : bflo(vb[d]);
                tw[c] = pack2(a1 * v1, a0 * v0);
            }
            const unsigned mp = 62 - t0;
#pragma unroll
            for (int c = 0; c < 8; ++c)
                *(unsigned LDSP*)(smw + K1_KT + (8 * oct + c) * 128 + (((mp >> 3) ^ oct) << 4) + (mp & 7) * 2) = tw[c];
            wait_lgkm0();
            lds_post(smw + CNT + 4 * D_TS, lane);
        }
        lds_wait(sm + CNT + 4 * D_TS, 4);
#pragma unroll
        for (int i = 0; i < 2; ++i) {
            const unsigned ch = 16 * kw + 8 * i + (lane >> 3), c = lane & 7;
            st16_sc1(rsWS, WS_UR + (j * 64 + ch) * 4096 + (1984 - 64 * tt + 8 * c) * 2, lds16(sm + K1_KT + ch * 128 + ((c ^ (ch >> 3)) << 4)));
        }
        lds_post(smw + CNT + 4 * D_TSF, lane);
        STAMP(2);
        wait_vm<0>();
        STAMP(3);
    } else {
        const int wm = w & 1, wn = w >> 1;
        const GemmAddr ga = gemm_addr(wm, wn, lane);
        f32x16 acc = {};
        lds_wait(sm + CNT + 4 * D_S0, 4); STAMP(1); gemm_stage(sm, ga, 0, acc);
        lds_wait(sm + CNT + 4 * D_S1, 4); gemm_stage(sm, ga, 2, acc);
        lds_wait(sm + CNT + 4 * D_S2, 4); gemm_stage(sm, ga, 4, acc);
        lds_wait(sm + CNT + 4 * D_S3, 4); gemm_chunk(sm, ga, 6, acc);
        lds_wait(sm + CNT + 4 * D_ALL, 4); gemm_chunk(sm, ga, 7, acc);
        STAMP(2);
        u32x2 p[4];
#pragma unroll
        for (int q = 0; q < 4; ++q) {
            const unsigned a = pack2(acc[4 * q], acc[4 * q + 1]), b = pack2(acc[4 * q + 2], acc[4 * q + 3]);
            p[q][0] = pack2(bflo(a) * QSCALE, bfhi(a) * QSCALE);
            p[q][1] = pack2(bflo(b) * QSCALE, bfhi(b) * QSCALE);
        }
        u32x4 r0, r1;
        quads_to_rows(p, r0, r1);
        lds_wait(sm + CNT + 4 * D_TSF, 4);
        {
            const unsigned row = 32 * wm + r, c0 = 4 * wn + h;
            *(u32x4 LDSP*)(smw + K1_KT + row * 128 + ((c0 ^ (row & 7)) << 4)) = r0;
            *(u32x4 LDSP*)(smw + K1_KT + row * 128 + (((c0 + 2) ^ (row & 7)) << 4)) = r1;
        }
        wait_lgkm0();
        lds_post(smw + CNT + 4 * D_QS, lane);
        lds_wait(sm + CNT + 4 * D_QS, 4);
#pragma unroll
        for (int i = 0; i < 2; ++i) {
            const unsigned row = 16 * w + 8 * i + (lane >> 3), c = lane & 7;
            st16_sc1(rsWS, WS_Q + (tt * 64 + row) * 1024 + j * 128 + c * 16, lds16(sm + K1_KT + row * 128 + ((c ^ (row & 7)) << 4)));
        }
        asm volatile("s_waitcnt vmcnt(0)" : "+v"(ep) :: "memory");
        STAMP(3);
    }
    const unsigned long long tag = ((unsigned long long)(unsigned)__builtin_amdgcn_readfirstlane((int)ep[1]) << 32 |
                                    (unsigned)__builtin_amdgcn_readfirstlane((int)ep[0])) + 1;
    const unsigned tlo = (unsigned)tag, thi = (unsigned)(tag >> 32);
    if (lds_inc(smw + CNT + 4 * D_P1, lane) == 11) {
        if (lane < NREP) st8_sc1(rsWS, WS_F1 + lane * 2048 + bid * 8, u32x2{tlo, thi});
    }
    STAMP(4);

    if (w < 8) {
        const int rg = w & 3, kh = w >> 2;
        u32x4 q0, q1;
        const unsigned qoff = WS_Q + (unsigned)(tb * 32 + 8 * rg + (r >> 3)) * 1024 + (8 * hg + (r & 7)) * 32 + h * 16;
        const unsigned kaddr = K2_K + kh * 32 * 1024 + lane * 16;
        const unsigned vaddr = K2_V + kh * 32 * 1088 + (r < 16 ? (h * 16 + r) * 16 : ((r & 11) == 0 ? 32 * 16 : 33 * 16));
        lds_wait(sm + CNT + 4 * C_GO, 1);
        bload16_sc1(q0, rsWS, qoff + kh * 4096);
        bload16_sc1(q1, rsWS, qoff + (kh ^ 1) * 4096);
        asm volatile("s_waitcnt vmcnt(0)" : "+v"(q0), "+v"(q1) :: "memory");
        f32x16 O0 = {}, O1 = {};
        STAMP(9);
        attn_fast(O0, O1, q0, q1, lds0 + kaddr, lds0 + vaddr, lds0 + CNT);
        STAMP(10);
        barrier();
        float m0 = 0.f, m1 = 0.f;
        const u32x4 slow4 = lds16(sm + CNT + 4 * C_SLOW);
        const bool anyslow = __builtin_amdgcn_readfirstlane((int)(slow4[0] | slow4[1] | slow4[2] | slow4[3])) != 0;
        bool slow = false;
        if (anyslow) {
            slow = __builtin_amdgcn_readfirstlane((int)lds_ld(sm + CNT + 4 * (C_SLOW + rg))) != 0;
            if (slow) {
                O0 = f32x16{}; O1 = f32x16{};
                m0 = m1 = -__builtin_inff();
                attn_slow(O0, m0, q0, sm, kaddr, vaddr);
                attn_slow(O1, m1, q1, sm, kaddr, vaddr);
            }
            barrier();
        }
        STAMP(11);
        {
            char LDSP* pw = smw + K2_PART + w * 2560;
            *(f32x4 LDSP*)(pw + lane * 16) = f32x4{O1[0], O1[1], O1[2], O1[3]};
            *(f32x4 LDSP*)(pw + 1024 + lane * 16) = f32x4{O1[4], O1[5], O1[6], O1[7]};
            *(f32x2 LDSP*)(pw + 2048 + lane * 8) = f32x2{O1[8], m1};
        }
        wait_lgkm0();
        barrier();
        STAMP(12);
        u32x4 o;
        {
            const char LDSP* pr = sm + K2_PART + (w ^ 4) * 2560;
            f32x4 pa = *(const f32x4 LDSP*)(pr + lane * 16), pb = *(const f32x4 LDSP*)(pr + 1024 + lane * 16);
            const f32x2 lm = *(const f32x2 LDSP*)(pr + 2048 + lane * 8);
            f32x4 ka = f32x4{O0[0], O0[1], O0[2], O0[3]}, kb = f32x4{O0[4], O0[5], O0[6], O0[7]};
            float l0 = O0[8], l1 = lm[0];
            if (slow) {
                const float mn = fmaxf(m0, lm[1]);
                const float s0 = __builtin_amdgcn_exp2f(m0 - mn), s1 = __builtin_amdgcn_exp2f(lm[1] - mn);
                ka *= s0; kb *= s0; l0 *= s0; pa *= s1; pb *= s1; l1 *= s1;
            }
            const float inv = __builtin_amdgcn_rcpf(l0 + l1);
            ka = (ka + pa) * inv; kb = (kb + pb) * inv;
            auto s0 = __builtin_amdgcn_permlane32_swap(pack2(ka[0], ka[1]), pack2(kb[0], kb[1]), false, false);
            auto s1 = __builtin_amdgcn_permlane32_swap(pack2(ka[2], ka[3]), pack2(kb[2], kb[3]), false, false);
            o = u32x4{min(s0[0], POISON - 1), min(s1[0], POISON - 1), min(s0[1], POISON - 1), min(s1[1], POISON - 1)};
        }
        const int tokl = 8 * rg + 4 * kh + (r >> 3);
        st16_sc1(rsWS, WS_O + (tb * 32 + tokl) * 1024 + (8 * hg + (r & 7)) * 32 + h * 16, o);
        *(u32x4 LDSP*)(smw + K2_OI + tokl * 1024 + ((((r & 7) * 2 + h) ^ (tokl & 15)) << 4)) = o;
        const int tk = 4 * w + (lane >> 4), c = lane & 15;
        const unsigned poff = WS_O + (tb * 32 + tk) * 1024 + c * 16;
        const unsigned po0 = poff + ((hg + 1) & 3) * 256, po1 = poff + ((hg + 2) & 3) * 256, po2 = poff + ((hg + 3) & 3) * 256;
        wait_lgkm0();
        barrier();
        STAMP(13);
        __builtin_amdgcn_s_sleep(XSLEEP);
        u32x4 p0, p1, p2;
        bload16_sc1(p0, rsWS, po0); bload16_sc1(p1, rsWS, po1); bload16_sc1(p2, rsWS, po2);
        asm volatile("s_waitcnt vmcnt(0)" : "+v"(p0), "+v"(p1), "+v"(p2) :: "memory");
        for (unsigned spins = 0; spins < (1u << 20); ++spins) {
            const unsigned mx = max(max(max(max(p0[0], p0[1]), max(p0[2], p0[3])), max(max(p1[0], p1[1]), max(p1[2], p1[3]))),
                                    max(max(p2[0], p2[1]), max(p2[2], p2[3])));
            if (__all(mx != POISON)) break;
            bload16_sc1(p0, rsWS, po0); bload16_sc1(p1, rsWS, po1); bload16_sc1(p2, rsWS, po2);
            asm volatile("s_waitcnt vmcnt(0)" : "+v"(p0), "+v"(p1), "+v"(p2) :: "memory");
        }
        *(u32x4 LDSP*)(smw + K2_OI + tk * 1024 + (((16 + c) ^ (tk & 15)) << 4)) = p0;
        *(u32x4 LDSP*)(smw + K2_OI + tk * 1024 + (((32 + c) ^ (tk & 15)) << 4)) = p1;
        *(u32x4 LDSP*)(smw + K2_OI + tk * 1024 + (((48 + c) ^ (tk & 15)) << 4)) = p2;
        STAMP(14);
        wait_lgkm0();
        barrier();
        STAMP(15);
    } else {
        const int hw = w - 8;
        __builtin_amdgcn_s_setprio(3);
        if (hw == 0) {
            const unsigned fo = WS_F1 + (bid & (NREP - 1)) * 2048 + lane * 32;
            for (unsigned spins = 0; spins < (1u << 20); ++spins) {
                u32x4 f0, f1;
                bload16_sc1(f0, rsWS, fo); bload16_sc1(f1, rsWS, fo + 16);
                asm volatile("s_waitcnt vmcnt(0)" : "+v"(f0), "+v"(f1) :: "memory");
                const unsigned dlo = (f0[0] ^ tlo) | (f0[2] ^ tlo) | (f1[0] ^ tlo) | (f1[2] ^ tlo);
                const unsigned dhi = (f0[1] ^ thi) | (f0[3] ^ thi) | (f1[1] ^ thi) | (f1[3] ^ thi);
                if (__all((dlo | dhi) == 0)) break;
                __builtin_amdgcn_s_sleep(POLLSLEEP);
            }
            lds_post(smw + CNT + 4 * C_GO, lane);
        } else {
            lds_wait(sm + CNT + 4 * C_GO, 1);
        }
        STAMP(8);
        const unsigned ko = hw < 2 ? hw * 32768 : K2_V + (hw - 2) * 34816;
        auto group = [&](int g) { dma4_sc1(rsWS, lane * 16, WS_KV + ko + g * 4096, lds0 + ko + g * 4096); };
        group(0); group(1);
        wait_vm<4>(); lds_post(smw + CNT + 4 * C_ST0, lane);
        group(2);
        wait_vm<4>(); lds_post(smw + CNT + 4 * C_ST1, lane);
        group(3);
        wait_vm<4>(); lds_post(smw + CNT + 4 * C_ST2, lane);
        group(4); group(5); group(6); group(7);
        if (hw >= 2) dma2_sc1(rsWS, lane * 16, WS_KV + ko + 32768, lds0 + ko + 32768);
        const int ch = hw >> 1, st = hw & 1;
        dma4(mk_rsrc(ssmk, DM * S_LEN * 2), st * 2 + lane * 16, (bid * 2 + ch) * 4096, lds0 + K2_H + (ch * 2 + st) * K2_HC + 32);
        dma2_sc1(rsWS, lane * 16, WS_UR + (bid * 2 + ch) * 4096 + st * 2048, lds0 + K2_U + ch * K2_UC + st * 2048);
        u32x4 q0, q1; u32x2 kn, vm;
        {
            const unsigned qoff = WS_Q + (unsigned)(tb * 32 + 8 * hw + (r >> 3)) * 1024 + (8 * hg + (r & 7)) * 32 + h * 16;
            bload16_sc1(q0, rsWS, qoff); bload16_sc1(q1, rsWS, qoff + 4096);
            bload8_sc1(kn, rsWS, WS_KN + lane * 8); bload8_sc1(vm, rsWS, WS_KN + 512 + lane * 8);
        }
        wait_vm<10>(); lds_post(smw + CNT + 4 * C_ST3, lane);
        wait_vm<4>();  lds_post(smw + CNT + 4 * (C_CH0 + ch), lane);
        asm volatile("s_waitcnt vmcnt(0)" : "+v"(q0), "+v"(q1), "+v"(kn), "+v"(vm) :: "memory");
        {
            const float kk = u2f(wave_umax(max(kn[0], kn[1]))), vx = u2f(wave_umax(max(vm[0], vm[1])));
            float qq0 = 0.f, qq1 = 0.f;
#pragma unroll
            for (int i = 0; i < 4; ++i) { qq0 = dot2(q0[i], q0[i], qq0); qq1 = dot2(q1[i], q1[i], qq1); }
            float qq = fmaxf(qq0 + __shfl_xor(qq0, 32), qq1 + __shfl_xor(qq1, 32));
            if (!(qq0 == qq0) || !(qq1 == qq1)) qq = __builtin_inff();
            const bool fast = __all(qq * kk <= 3600.f && vx <= 1.0e12f);
            if (lane == 0) *(unsigned LDSP*)(smw + CNT + 4 * (C_SLOW + hw)) = fast ? 0u : 1u;
        }
        lds_wait(sm + CNT + 4 * (C_CH0 + ch), 2);
        __builtin_amdgcn_s_setprio(2);
        STAMP(9);
        {
            const u16 h0 = *(const u16 LDSP*)(sm + K2_H + (ch * 2) * K2_HC + 32);
            if (lane < 16) {
                *(u16 LDSP*)(smw + K2_H + (ch * 2) * K2_HC + lane * 2) = 0;
                *(u16 LDSP*)(smw + K2_H + (ch * 2 + 1) * K2_HC + lane * 2) = lane == 15 ? h0 : (u16)0;
            }
            if (lane < 32) *(u32x4 LDSP*)(smw + K2_U + ch * K2_UC + 4096 + lane * 16) = u32x4{0, 0, 0, 0};
            wait_lgkm0();
            const float dskip = bflo((unsigned)ssmD[bid * 2 + ch]);
            const unsigned yoff = WS_YT + (bid * 2 + ch) * 4096;
            if (st) conv_wave<1>(sm, ch, lane, dskip, rsWS, yoff); else conv_wave<0>(sm, ch, lane, dskip, rsWS, yoff);
        }
        STAMP(10);
        if (bid == 0 && hw == 0 && lane == 0) st8_sc1(rsWS, WS_EP, u32x2{tlo, thi});
        wait_vm<0>();
        if (lds_inc(smw + CNT + 4 * C_YST, lane) == 3) {
            if (lane == 0) st8_sc1(rsWS, WS_FY + bid * 8, u32x2{tlo, thi});
        }
        lds_wait(sm + CNT + 4 * C_YST, 4);
        const int nt = 4 * hg + hw;
        {
            const rsrc_t rsX = mk_rsrc(x, S_LEN * DM * 2);
            const unsigned vxo = (tb * 32 + (lane >> 2)) * 1024 + nt * 64 + (lane & 3) * 16;
            dma1(rsX, vxo, 0, lds0 + K2_XP + hw * 2048);
            dma1(rsX, vxo, 16 * 1024, lds0 + K2_XP + hw * 2048 + 1024);
        }
        const float g = u2f(__builtin_amdgcn_raw_buffer_load_b32(rsWS, WS_G + (tb * 32 + r) * 4, 0, 16));
        u32x4 wf[32];
#pragma unroll
        for (int i = 0; i < 32; ++i) wf[i] = ld16_sc1(rsWS, WS_WO + (nt * 32 + ((8 * hg + i) & 31)) * 1024 + lane * 16);
        {
            for (unsigned spins = 0; spins < (1u << 20); ++spins) {
                u32x2 f;
                bload8_sc1(f, rsWS, WS_FY + (16 * nt + (lane & 15)) * 8);
                asm volatile("s_waitcnt vmcnt(0)" : "+v"(f) :: "memory");
                if (__all(f[0] == tlo && f[1] == thi)) break;
                __builtin_amdgcn_s_sleep(32);
            }
#pragma unroll
            for (int i = 0; i < 2; ++i) {
                const u32x4 yv = ld16_sc1(rsWS, WS_YT + (nt * 32 + 16 * i + (lane >> 2)) * 4096 + tb * 64 + (lane & 3) * 16);
                *(u32x4 LDSP*)(smw + K2_YP + hw * 2048 + i * 1024 + lane * 16) = yv;
            }
        }
        wait_vm<0>();
        wait_lgkm0();
        u32x2 xy[4];
        {
            const unsigned G = lane >> 4, qq = (lane & 15) >> 2, pp = lane & 3;
#pragma unroll
            for (int q = 0; q < 4; ++q) {
                const u32x2 y = ldstr(sm + K2_YP + hw * 2048 + (8 * q + 4 * (G >> 1) + qq) * 64 + (16 * (G & 1) + 4 * pp) * 2);
                const u32x2 xx = *(const u32x2 LDSP*)(sm + K2_XP + hw * 2048 + r * 64 + (8 * q + 4 * h) * 2);
                xy[q] = u32x2{pack2(bflo(xx[0]) * bflo(y[0]) * g, bfhi(xx[0]) * bfhi(y[0]) * g), pack2(bflo(xx[1]) * bflo(y[1]) * g, bfhi(xx[1]) * bfhi(y[1]) * g)};
            }
        }
        float g1 = 1.f - g;
        asm volatile("" : "+v"(xy[0]), "+v"(xy[1]), "+v"(xy[2]), "+v"(xy[3]), "+v"(g1));
        STAMP(11);
        barrier();
        {
            const u32x4 slow4 = lds16(sm + CNT + 4 * C_SLOW);
            if (__builtin_amdgcn_readfirstlane((int)(slow4[0] | slow4[1] | slow4[2] | slow4[3])) != 0) barrier();
        }
        barrier();
        barrier();
        f32x16 acc = {};
#pragma unroll
        for (int i = 0; i < 8; ++i) acc = mfma32(wf[i], lds16(sm + K2_OI + r * 1024 + (((2 * i + h) ^ (r & 15)) << 4)), acc);
        const unsigned ob = lds0 + K2_OI + r * 1024 + ((h ^ (r & 15)) << 4);
        STAMP(12);
        barrier();
        STAMP(13);
        oproj12<8>(acc, wf + 8, ob);
        oproj12<20>(acc, wf + 20, ob);
        STAMP(14);
        u32x2 p[4];
#pragma unroll
        for (int q = 0; q < 4; ++q) {
            p[q][0] = pack2(fmaf(g1, acc[4 * q], bflo(xy[q][0])), fmaf(g1, acc[4 * q + 1], bfhi(xy[q][0])));
            p[q][1] = pack2(fmaf(g1, acc[4 * q + 2], bflo(xy[q][1])), fmaf(g1, acc[4 * q + 3], bfhi(xy[q][1])));
        }
        u32x4 b0, b1;
        quads_to_rows(p, b0, b1);
        char* op = (char*)out + (size_t)(tb * 32 + r) * 1024 + nt * 64 + h * 16;
        *(u32x4*)op = b0;
        *(u32x4*)(op + 32) = b1;
        STAMP(15);
    }
    STAMP_FLUSH();
}

extern "C" void kernel_launch(void* const* d_in, const int* in_sizes, int n_in, void* d_out, int out_size, void* d_ws, size_t ws_size,
                              hipStream_t stream) {
    gm_fused<<<256, 768, 0, stream>>>((const u16*)d_in[0], (const u16*)d_in[1], (const u16*)d_in[2], (const u16*)d_in[3], (const u16*)d_in[4],
                                      (const u16*)d_in[5], (const u16*)d_in[7], (const u16*)d_in[8], (const u16*)d_in[9], (const u16*)d_in[10],
                                      (const u16*)d_in[11], (char*)d_ws, (u16*)d_out);
}
```

```cpp
#include <hip/hip_runtime.h>
#include <hip/hip_bf16.h>
#include <stdint.h>

typedef unsigned short u16;

namespace {

typedef __attribute__((ext_vector_type(8))) __bf16 bf16x8;
typedef __attribute__((ext_vector_type(16))) __bf16 bf16x16;
typedef __attribute__((ext_vector_type(2))) __bf16 bf16x2;
typedef __attribute__((ext_vector_type(4))) short s16x4;
typedef __attribute__((ext_vector_type(2))) float f32x2;
typedef __attribute__((ext_vector_type(4))) float f32x4;
typedef __attribute__((ext_vector_type(8))) float f32x8;
typedef __attribute__((ext_vector_type(16))) float f32x16;
typedef __attribute__((ext_vector_type(2))) unsigned u32x2;
typedef __attribute__((ext_vector_type(4))) unsigned u32x4;
typedef __amdgpu_buffer_rsrc_t rsrc_t;
#define LDSP __attribute__((address_space(3)))

constexpr int S_LEN = 2048, DM = 512;

constexpr unsigned WS_Q  = 0;
constexpr unsigned WS_UR = 2u << 20;
constexpr unsigned WS_O  = 4u << 20;
constexpr unsigned WS_YT = 6u << 20;
constexpr unsigned WS_KV = 8u << 20;
constexpr unsigned KF_BYTES = 65536, VF_BYTES = 64 * 1088, KV_BYTES = KF_BYTES + VF_BYTES;
constexpr unsigned WS_KN = WS_KV + KV_BYTES;
constexpr unsigned WS_G  = WS_KN + 1024;
#define NREPL 8
constexpr unsigned NREP = NREPL;
constexpr unsigned WS_F1 = WS_G + 8192;
constexpr unsigned WS_FY = WS_F1 + NREP * 2048;
constexpr unsigned WS_EP = WS_FY + 2048;
constexpr unsigned WS_WO = 9u << 20;
constexpr unsigned WS_END = WS_WO + (512u << 10);
constexpr unsigned POISON = 0xffffffffu;

#define STAMP_DECL
#define STAMP(i)
#define STAMP_FLUSH()
constexpr float QSCALE = 0.25f * 1.4426950408889634f;

__device__ __forceinline__ rsrc_t mk_rsrc(const void* p, unsigned bytes) {
    return __builtin_amdgcn_make_buffer_rsrc((void*)p, (short)0, (int)bytes, 0x00020000);
}
__device__ __forceinline__ void dma16(rsrc_t rs, unsigned voff, unsigned soff, unsigned ldsaddr) {
    asm volatile("s_nop 4\n\ts_mov_b32 m0, %0\n\ts_nop 0\n\tbuffer_load_dwordx4 %1, %2, %3 offen lds"
                 :: "s"(ldsaddr), "v"(voff), "s"(rs), "s"(soff) : "memory");
}
__device__ __forceinline__ void bload16(u32x4& dst, rsrc_t rs, unsigned voff) {
    asm volatile("buffer_load_dwordx4 %0, %1, %2, 0 offen" : "=v"(dst) : "v"(voff), "s"(rs) : "memory");
}
__device__ __forceinline__ void bload16_sc1(u32x4& dst, rsrc_t rs, unsigned voff) {
    asm volatile("buffer_load_dwordx4 %0, %1, %2, 0 offen sc1" : "=v"(dst) : "v"(voff), "s"(rs) : "memory");
}
__device__ __forceinline__ void bload8_sc1(u32x2& dst, rsrc_t rs, unsigned voff) {
    asm volatile("buffer_load_dwordx2 %0, %1, %2, 0 offen sc1" : "=v"(dst) : "v"(voff), "s"(rs) : "memory");
}
__device__ __forceinline__ void bload4(unsigned& dst, rsrc_t rs, unsigned voff) {
    asm volatile("buffer_load_dword %0, %1, %2, 0 offen" : "=v"(dst) : "v"(voff), "s"(rs) : "memory");
}
__device__ __forceinline__ void st16_sc1(rsrc_t rs, unsigned voff, u32x4 v) { __builtin_amdgcn_raw_buffer_store_b128(v, rs, voff, 0, 16); }
__device__ __forceinline__ void st4_sc1(rsrc_t rs, unsigned voff, unsigned v) { __builtin_amdgcn_raw_buffer_store_b32(v, rs, voff, 0, 16); }
__device__ __forceinline__ void st8_sc1(rsrc_t rs, unsigned voff, u32x2 v) { __builtin_amdgcn_raw_buffer_store_b64(v, rs, voff, 0, 16); }
__device__ __forceinline__ u32x4 ld16_sc1(rsrc_t rs, unsigned voff) { return __builtin_amdgcn_raw_buffer_load_b128(rs, voff, 0, 16); }
template <int N> __device__ __forceinline__ void wait_vm() { asm volatile("s_waitcnt vmcnt(%0)" :: "i"(N) : "memory"); }
__device__ __forceinline__ void wait_lgkm0() { asm volatile("s_waitcnt lgkmcnt(0)" ::: "memory"); }
__device__ __forceinline__ void barrier() { asm volatile("" ::: "memory"); __builtin_amdgcn_s_barrier(); asm volatile("" ::: "memory"); }

__device__ __forceinline__ unsigned pack2(float a, float b) {
    f32x2 v = {a, b};
    return __builtin_bit_cast(unsigned, __builtin_convertvector(v, bf16x2));
}
__device__ __forceinline__ float u2f(unsigned u) { return __builtin_bit_cast(float, u); }
__device__ __forceinline__ float bflo(unsigned u) { return __builtin_bit_cast(float, u << 16); }
__device__ __forceinline__ float bfhi(unsigned u) { return __builtin_bit_cast(float, u & 0xffff0000u); }
__device__ __forceinline__ float rbf(float a) { return bflo(pack2(a, 0.f)); }

__device__ __forceinline__ u32x4 lds16(const char LDSP* p) { return *(const u32x4 LDSP*)p; }
__device__ __forceinline__ u32x2 ldstr(const char LDSP* p) {
    return __builtin_bit_cast(u32x2, __builtin_amdgcn_ds_read_tr16_b64_v4i16((s16x4 LDSP*)p));
}
__device__ __forceinline__ bf16x8 asbf(u32x4 v) { return __builtin_bit_cast(bf16x8, v); }
__device__ __forceinline__ u32x4 cat2(u32x2 a, u32x2 b) { return u32x4{a[0], a[1], b[0], b[1]}; }
__device__ __forceinline__ f32x16 mfma32(u32x4 a, u32x4 b, f32x16 c) { return __builtin_amdgcn_mfma_f32_32x32x16_bf16(asbf(a), asbf(b), c, 0, 0, 0); }
__device__ __forceinline__ f32x4 mfma16(u32x4 a, u32x4 b, f32x4 c) { return __builtin_amdgcn_mfma_f32_16x16x32_bf16(asbf(a), asbf(b), c, 0, 0, 0); }

constexpr unsigned G_XS = 0, G_WS = 65536;

struct GemmAddr { unsigned xa[4]; unsigned wa; };
__device__ __forceinline__ GemmAddr gemm_addr(int wm, int wn, int lane) {
    GemmAddr g;
    const unsigned row = 32 * wm + (lane & 31), h = lane >> 5;
#pragma unroll
    for (int s = 0; s < 4; ++s) g.xa[s] = G_XS + row * 128 + (((2 * s + h) ^ ((row >> 1) & 7)) << 4);
    const unsigned G = lane >> 4, q = (lane & 15) >> 2, p = lane & 3;
    const unsigned c = 4 * wn + 2 * (G & 1) + (p >> 1);
    g.wa = G_WS + (8 * (G >> 1) + q) * 128 + ((c ^ (((q >> 1) & 1) << 2)) << 4) + 8 * (p & 1);
    return g;
}
__device__ __forceinline__ void gemm_chunk(const char LDSP* sm, const GemmAddr& g, int kc, f32x16& acc) {
    u32x4 xb[4]; u32x2 w0[4], w1[4];
#pragma unroll
    for (int s = 0; s < 4; ++s) {
        xb[s] = lds16(sm + g.xa[s] + kc * 8192);
        w0[s] = ldstr(sm + g.wa + kc * 8192 + s * 2048);
        w1[s] = ldstr(sm + g.wa + kc * 8192 + s * 2048 + 512);
    }
#pragma unroll
    for (int s = 0; s < 4; ++s) acc = mfma32(cat2(w0[s], w1[s]), xb[s], acc);
}
__device__ __forceinline__ void gemm_stage(const char LDSP* sm, const GemmAddr& g, int kc0, f32x16& acc) {
    u32x4 xb[8]; u32x2 w0[8], w1[8];
#pragma unroll
    for (int s = 0; s < 8; ++s) {
        const int kc = kc0 + (s >> 2), ss = s & 3;
        xb[s] = lds16(sm + g.xa[ss] + kc * 8192);
        w0[s] = ldstr(sm + g.wa + kc * 8192 + ss * 2048);
        w1[s] = ldstr(sm + g.wa + kc * 8192 + ss * 2048 + 512);
    }
#pragma unroll
    for (int s = 0; s < 8; ++s) acc = mfma32(cat2(w0[s], w1[s]), xb[s], acc);
}
__device__ __forceinline__ void quads_to_rows(u32x2 (&p)[4], u32x4& r0, u32x4& r1) {
    auto s0 = __builtin_amdgcn_permlane32_swap(p[0][0], p[1][0], false, false);
    auto s1 = __builtin_amdgcn_permlane32_swap(p[0][1], p[1][1], false, false);
    auto s2 = __builtin_amdgcn_permlane32_swap(p[2][0], p[3][0], false, false);
    auto s3 = __builtin_amdgcn_permlane32_swap(p[2][1], p[3][1], false, false);
    r0 = u32x4{s0[0], s1[0], s0[1], s1[1]};
    r1 = u32x4{s2[0], s3[0], s2[1], s3[1]};
}
__device__ __forceinline__ unsigned wave_umax(unsigned v) {
    v = max(v, (unsigned)__builtin_amdgcn_update_dpp(0, (int)v, 0x111, 0xf, 0xf, true));
    v = max(v, (unsigned)__builtin_amdgcn_update_dpp(0, (int)v, 0x112, 0xf, 0xf, true));
    v = max(v, (unsigned)__builtin_amdgcn_update_dpp(0, (int)v, 0x114, 0xf, 0xf, true));
    v = max(v, (unsigned)__builtin_amdgcn_update_dpp(0, (int)v, 0x118, 0xf, 0xf, true));
    v = max(v, (unsigned)__builtin_amdgcn_update_dpp(0, (int)v, 0x142, 0xa, 0xf, false));
    v = max(v, (unsigned)__builtin_amdgcn_update_dpp(0, (int)v, 0x143, 0xc, 0xf, false));
    return (unsigned)__builtin_amdgcn_readlane((int)v, 63);
}

__device__ __forceinline__ unsigned lds_ld(const char LDSP* p) { return *(const volatile unsigned LDSP*)p; }
__device__ __forceinline__ void lds_wait(const char LDSP* p, unsigned n) {
    while ((unsigned)__builtin_amdgcn_readfirstlane((int)lds_ld(p)) < n) __builtin_amdgcn_s_sleep(1);
    asm volatile("" ::: "memory");
}
__device__ __forceinline__ void lds_post(char LDSP* p, int lane) {
    asm volatile("" ::: "memory");
    if (lane == 0) __hip_atomic_fetch_add((unsigned LDSP*)p, 1u, __ATOMIC_RELAXED, __HIP_MEMORY_SCOPE_WORKGROUP);
    asm volatile("" ::: "memory");
}
__device__ __forceinline__ unsigned lds_inc(char LDSP* p, int lane) {
    unsigned old = 0;
    asm volatile("" ::: "memory");
    if (lane == 0) old = __hip_atomic_fetch_add((unsigned LDSP*)p, 1u, __ATOMIC_RELAXED, __HIP_MEMORY_SCOPE_WORKGROUP);
    old = (unsigned)__builtin_amdgcn_readfirstlane((int)old);
    asm volatile("" ::: "memory");
    return old;
}
__device__ __forceinline__ void dma1(rsrc_t rs, unsigned voff, unsigned soff, unsigned ldsaddr) {
    asm volatile("s_mov_b32 m0, %0\n\ts_nop 0\n\tbuffer_load_dwordx4 %1, %2, %3 offen lds"
                 :: "s"(ldsaddr), "v"(voff), "s"(rs), "s"(soff) : "memory");
}
__device__ __forceinline__ void dma2(rsrc_t rs, unsigned voff, unsigned soff, unsigned ldsaddr) {
    asm volatile("s_mov_b32 m0, %0\n\ts_nop 0\n\tbuffer_load_dwordx4 %1, %2, %3 offen lds\n\tbuffer_load_dwordx4 %1, %2, %3 offen offset:1024 lds"
                 :: "s"(ldsaddr), "v"(voff), "s"(rs), "s"(soff) : "memory");
}
__device__ __forceinline__ void dma4(rsrc_t rs, unsigned voff, unsigned soff, unsigned ldsaddr) {
    asm volatile("s_mov_b32 m0, %0\n\ts_nop 0\n\tbuffer_load_dwordx4 %1, %2, %3 offen lds\n\tbuffer_load_dwordx4 %1, %2, %3 offen offset:1024 lds\n\t"
                 "buffer_load_dwordx4 %1, %2, %3 offen offset:2048 lds\n\tbuffer_load_dwordx4 %1, %2, %3 offen offset:3072 lds"
                 :: "s"(ldsaddr), "v"(voff), "s"(rs), "s"(soff) : "memory");
}
__device__ __forceinline__ void dma1_sc1(rsrc_t rs, unsigned voff, unsigned soff, unsigned ldsaddr) {
    asm volatile("s_mov_b32 m0, %0\n\ts_nop 0\n\tbuffer_load_dwordx4 %1, %2, %3 offen sc1 lds"
                 :: "s"(ldsaddr), "v"(voff), "s"(rs), "s"(soff) : "memory");
}
__device__ __forceinline__ void dma2_sc1(rsrc_t rs, unsigned voff, unsigned soff, unsigned ldsaddr) {
    asm volatile("s_mov_b32 m0, %0\n\ts_nop 0\n\tbuffer_load_dwordx4 %1, %2, %3 offen sc1 lds\n\tbuffer_load_dwordx4 %1, %2, %3 offen offset:1024 sc1 lds"
                 :: "s"(ldsaddr), "v"(voff), "s"(rs), "s"(soff) : "memory");
}
__device__ __forceinline__ void dma4_sc1(rsrc_t rs, unsigned voff, unsigned soff, unsigned ldsaddr) {
    asm volatile("s_mov_b32 m0, %0\n\ts_nop 0\n\tbuffer_load_dwordx4 %1, %2, %3 offen sc1 lds\n\tbuffer_load_dwordx4 %1, %2, %3 offen offset:1024 sc1 lds\n\t"
                 "buffer_load_dwordx4 %1, %2, %3 offen offset:2048 sc1 lds\n\tbuffer_load_dwordx4 %1, %2, %3 offen offset:3072 sc1 lds"
                 :: "s"(ldsaddr), "v"(voff), "s"(rs), "s"(soff) : "memory");
}
__device__ __forceinline__ float dot2(unsigned a, unsigned b, float c) {
    return __builtin_amdgcn_fdot2_f32_bf16(__builtin_bit_cast(bf16x2, a), __builtin_bit_cast(bf16x2, b), c, false);
}

constexpr unsigned K1_WE = 131072;
constexpr unsigned K1_KT = K1_WE + 16384;
constexpr unsigned K1_WO = K1_KT + 8192;
constexpr unsigned K2_K = 0, K2_V = 65536, K2_H = KV_BYTES, K2_HC = 4160, K2_U = K2_H + 4 * K2_HC, K2_UC = 4608;
constexpr unsigned K2_CNT = K2_U + 2 * K2_UC;
constexpr unsigned K2_LDS = K2_CNT + 128;
constexpr unsigned K2_XP = K2_H, K2_YP = K2_H + 8192;
constexpr unsigned K2_PART = 0;
constexpr unsigned K2_OI = 32768;
#define XSLEEP 10
enum { C_ST0 = 0, C_ST1 = 1, C_ST2 = 2, C_ST3 = 3, C_ST4 = 4, C_CH0 = 5, C_CH1 = 6, C_YST = 7, C_SLOW = 8  ,
       D_S0 = 12, D_S1 = 13, D_S2 = 14, D_S3 = 15, D_ALL = 16, D_TS = 17, D_P1 = 18, D_TSF = 19, D_QS = 20, D_LD = 21, C_QOK = 22, C_GOB = 23 };
static_assert(K1_WO + 2048 <= K2_CNT, "phase 1 images overlap the counters");

#define AE(X, i) "v_exp_f32 v[" #X "+" #i "], v[" #X "+" #i "]\n\t"
#define ASTEP(X0, X1, Y0, Y1, t)                                                             \
    AE(X0, 0) AE(X0, 1) AE(X0, 2) AE(X0, 3)                                                  \
    "s_waitcnt lgkmcnt(2)\n\t"                                                               \
    "v_mfma_f32_32x32x16_bf16 v[" #Y0 ":" #Y0 "+15], %2, %10, 0\n\t"                         \
    AE(X0, 4) AE(X0, 5) AE(X0, 6) AE(X0, 7)                                                  \
    "v_mfma_f32_32x32x16_bf16 v[" #Y1 ":" #Y1 "+15], %2, %11, 0\n\t"                         \
    "ds_read_b128 %2, %5 offset:(" #t "+2)*1024\n\t"                                         \
    AE(X0, 8) AE(X0, 9) AE(X0, 10) AE(X0, 11)                                                \
    "s_waitcnt lgkmcnt(2)\n\t"                                                               \
    "v_smfmac_f32_32x32x32_bf16 %0, %3, v[" #X0 ":" #X0 "+7], %12\n\t"                       \
    AE(X0, 12) AE(X0, 13) AE(X0, 14) AE(X0, 15) AE(X1, 0) AE(X1, 1)                          \
    "s_waitcnt lgkmcnt(1)\n\t"                                                               \
    "v_smfmac_f32_32x32x32_bf16 %0, %4, v[" #X0 "+8:" #X0 "+15], %12\n\t"                    \
    AE(X1, 2) AE(X1, 3) AE(X1, 4) AE(X1, 5) AE(X1, 6) AE(X1, 7) AE(X1, 8) AE(X1, 9)          \
    "v_smfmac_f32_32x32x32_bf16 %1, %3, v[" #X1 ":" #X1 "+7], %12\n\t"                       \
    "ds_read_b128 %3, %6 offset:(" #t "+1)*1088\n\t"                                         \
    AE(X1, 10) AE(X1, 11) AE(X1, 12) AE(X1, 13) AE(X1, 14) AE(X1, 15)                        \
    "s_nop 1\n\t"                                                                            \
    "v_smfmac_f32_32x32x32_bf16 %1, %4, v[" #X1 "+8:" #X1 "+15], %12\n\t"                    \
    "ds_read_b128 %4, %6 offset:(" #t "+1)*1088+544\n\t"
#define ASTEP_E(t) ASTEP(0, 16, 32, 48, t)
#define ASTEP_O(t) ASTEP(32, 48, 0, 16, t)
#define AWAIT(lbl, off)                                                                      \
    #lbl ":\n\t"                                                                             \
    "ds_read_b32 %7, %13 offset:" #off "\n\t"                                                \
    "s_waitcnt lgkmcnt(0)\n\t"                                                               \
    "v_readfirstlane_b32 %8, %7\n\t"                                                         \
    "s_nop 3\n\t"                                                                            \
    "s_cmp_ge_u32 %8, 4\n\t"                                                                 \
    "s_cbranch_scc1 " #lbl "9f\n\t"                                                          \
    "s_sleep 1\n\t"                                                                          \
    "s_branch " #lbl "b\n\t"                                                                 \
    #lbl "9:\n\t"

__device__ __forceinline__ void attn_fast(f32x16& O0, f32x16& O1, u32x4 q0, u32x4 q1, unsigned kaddr, unsigned vaddr, unsigned caddr) {
    u32x4 kf, v0, v1;
    unsigned idx = 0xddddu, tv; int ts, it = 0;
    asm volatile(
        "s_setprio 2\n\t"
        AWAIT(1, 0)
        "ds_read_b128 %2, %5\n\t"
        "s_waitcnt lgkmcnt(0)\n\t"
        "v_mfma_f32_32x32x16_bf16 v[0:15], %2, %10, 0\n\t"
        "v_mfma_f32_32x32x16_bf16 v[16:31], %2, %11, 0\n\t"
        "ds_read_b128 %2, %5 offset:1024\n\t"
        "ds_read_b128 %3, %6\n\t"
        "ds_read_b128 %4, %6 offset:544\n\t"
        "s_nop 10\n\t"
        "4:\n\t"
        ASTEP_E(0) ASTEP_O(1)
        "s_cmp_gt_u32 %9, 1\n\t"
        "s_cbranch_scc1 5f\n\t"
        "s_cmp_eq_u32 %9, 0\n\t"
        "s_cbranch_scc0 7f\n\t"
        AWAIT(2, 4)
        "s_branch 5f\n\t"
        "7:\n\t"
        AWAIT(8, 12)
        "5:\n\t"
        ASTEP_E(2) ASTEP_O(3) ASTEP_E(4) ASTEP_O(5)
        "s_cmp_gt_u32 %9, 1\n\t"
        "s_cbranch_scc1 6f\n\t"
        "s_cmp_eq_u32 %9, 0\n\t"
        "s_cbranch_scc0 12f\n\t"
        AWAIT(3, 8)
        "s_branch 6f\n\t"
        "12:\n\t"
        AWAIT(13, 16)
        "6:\n\t"
        ASTEP_E(6) ASTEP_O(7)
        "v_add_u32 %5, 0x2000, %5\n\t"
        "v_add_u32 %6, 0x2200, %6\n\t"
        "s_add_u32 %9, %9, 1\n\t"
        "s_cmp_eq_u32 %9, 2\n\t"
        "s_cbranch_scc0 10f\n\t"
        "s_setprio 1\n\t"
        "10:\n\t"
        "s_cmp_eq_u32 %9, 3\n\t"
        "s_cbranch_scc0 11f\n\t"
        "s_setprio 0\n\t"
        "11:\n\t"
        "s_cmp_lg_u32 %9, 4\n\t"
        "s_cbranch_scc1 4b\n\t"
        "s_waitcnt lgkmcnt(0)\n\t"
        "s_nop 15\n\t"
        "s_nop 7\n\t"
        : "+v"(O0), "+v"(O1), "=&v"(kf), "=&v"(v0), "=&v"(v1), "+v"(kaddr), "+v"(vaddr), "=&v"(tv), "=&s"(ts), "+s"(it)
        : "v"(q0), "v"(q1), "v"(idx), "v"(caddr)
        : "v0", "v1", "v2", "v3", "v4", "v5", "v6", "v7", "v8", "v9", "v10", "v11", "v12", "v13", "v14", "v15",
          "v16", "v17", "v18", "v19", "v20", "v21", "v22", "v23", "v24", "v25", "v26", "v27", "v28", "v29", "v30", "v31",
          "v32", "v33", "v34", "v35", "v36", "v37", "v38", "v39", "v40", "v41", "v42", "v43", "v44", "v45", "v46", "v47",
          "v48", "v49", "v50", "v51", "v52", "v53", "v54", "v55", "v56", "v57", "v58", "v59", "v60", "v61", "v62", "v63",
          "scc", "memory");
}

__device__ __forceinline__ void attn_slow(f32x16& O, float& m, u32x4 q, const char LDSP* sm, unsigned kaddr, unsigned vaddr) {
#pragma unroll 1
    for (int t = 0; t < 32; ++t) {
        const u32x4 kf = lds16(sm + kaddr + t * 1024);
        const u32x4 v0 = lds16(sm + vaddr + t * 1088), v1 = lds16(sm + vaddr + t * 1088 + 544);
        f32x16 s = mfma32(kf, q, f32x16{});
        float tm = s[0];
#pragma unroll
        for (int r = 1; r < 16; ++r) tm = fmaxf(tm, s[r]);
        tm = fmaxf(tm, __shfl_xor(tm, 32));
        const float mn = fmaxf(m, tm);
        const float alpha = __builtin_amdgcn_exp2f(m - mn);
        m = mn;
#pragma unroll
        for (int r = 0; r < 16; ++r) { O[r] *= alpha; s[r] = __builtin_amdgcn_exp2f(s[r] - mn); }
        const f32x8 slo = __builtin_shufflevector(s, s, 0, 1, 2, 3, 4, 5, 6, 7), shi = __builtin_shufflevector(s, s, 8, 9, 10, 11, 12, 13, 14, 15);
        O = __builtin_amdgcn_smfmac_f32_32x32x32_bf16(asbf(v0), __builtin_bit_cast(bf16x16, slo), O, 0xdddd, 0, 0);
        O = __builtin_amdgcn_smfmac_f32_32x32x32_bf16(asbf(v1), __builtin_bit_cast(bf16x16, shi), O, 0xdddd, 0, 0);
    }
}

template <int SET>
__device__ __forceinline__ void conv_wave(const char LDSP* sm, int ch, int lane, float dskip, rsrc_t rsWS, unsigned yoff) {
    constexpr int T0 = SET ? 1 : 0, T1 = SET ? 2 : 3, T2 = SET ? 5 : 4, T3 = SET ? 6 : 7;
    constexpr int NP = T3 + 1;
    const int b = lane & 15, g = lane >> 4;
    const int c = (b + 1) & 1;
    unsigned ha[4];
#pragma unroll
    for (int i = 0; i < 4; ++i) {
        ha[i] = K2_H + (ch * 2 + c) * K2_HC + ((b + 1 - c) / 2 + 4 * g) * 4 + i * 1024;
        asm volatile("" : "+v"(ha[i]));
    }
    unsigned ua = K2_U + ch * K2_UC + (2032 - 16 * b + 8 * g) * 2 - 7 * 512;
    asm volatile("" : "+v"(ua));
    f32x4 acc[4] = {};
#pragma unroll
    for (int r = 0; r < 8; ++r) {
        u32x4 A[NP], B[NP];
#pragma unroll
        for (int p = 0; p < NP; ++p) {
            const unsigned LDSP* hp = (const unsigned LDSP*)(sm + ha[p >> 1] + r * 64 + (p & 1) * 512);
            A[p] = u32x4{hp[0], hp[1], hp[2], hp[3]};
            B[p] = lds16(sm + ua + r * 64 + (7 - p) * 512);
        }
#pragma unroll
        for (int p = 0; p < NP; ++p) {
            if (p <= T0) acc[0] = mfma16(A[p], B[T0 - p < 0 ? 0 : T0 - p], acc[0]);
            if (p <= T1) acc[1] = mfma16(A[p], B[T1 - p < 0 ? 0 : T1 - p], acc[1]);
            if (p <= T2) acc[2] = mfma16(A[p], B[T2 - p < 0 ? 0 : T2 - p], acc[2]);
            if (p <= T3) acc[3] = mfma16(A[p], B[T3 - p], acc[3]);
        }
    }
    constexpr int TI[4] = {T0, T1, T2, T3};
#pragma unroll
    for (int i = 0; i < 4; ++i) {
        const int s = 256 * TI[i] + 16 * b + 4 * g;
        const u32x2 u = *(const u32x2 LDSP*)(sm + K2_U + ch * K2_UC + (2044 - s) * 2);
        const float y0 = rbf(acc[i][0]) + rbf(bfhi(u[1]) * dskip), y1 = rbf(acc[i][1]) + rbf(bflo(u[1]) * dskip);
        const float y2 = rbf(acc[i][2]) + rbf(bfhi(u[0]) * dskip), y3 = rbf(acc[i][3]) + rbf(bflo(u[0]) * dskip);
        st8_sc1(rsWS, yoff + s * 2, u32x2{pack2(y0, y1), pack2(y2, y3)});
    }
}

template <int I0>
__device__ __forceinline__ void oproj12(f32x16& acc, const u32x4* wf, unsigned ob) {
    u32x4 t0, t1, t2, t3; unsigned a;
    asm volatile(
        "v_xor_b32 %5, (((%c19+0)&7)<<5), %18\n\t" "ds_read_b128 %1, %5 offset:((%c19+0)>>3)*256\n\t"
        "v_xor_b32 %5, (((%c19+1)&7)<<5), %18\n\t" "ds_read_b128 %2, %5 offset:((%c19+1)>>3)*256\n\t"
        "v_xor_b32 %5, (((%c19+2)&7)<<5), %18\n\t" "ds_read_b128 %3, %5 offset:((%c19+2)>>3)*256\n\t"
        "v_xor_b32 %5, (((%c19+3)&7)<<5), %18\n\t" "ds_read_b128 %4, %5 offset:((%c19+3)>>3)*256\n\t"
        "s_waitcnt lgkmcnt(3)\n\t" "v_mfma_f32_32x32x16_bf16 %0, %6, %1, %0\n\t"
        "v_xor_b32 %5, (((%c19+4)&7)<<5), %18\n\t" "ds_read_b128 %1, %5 offset:((%c19+4)>>3)*256\n\t"
        "s_waitcnt lgkmcnt(3)\n\t" "v_mfma_f32_32x32x16_bf16 %0, %7, %2, %0\n\t"
        "v_xor_b32 %5, (((%c19+5)&7)<<5), %18\n\t" "ds_read_b128 %2, %5 offset:((%c19+5)>>3)*256\n\t"
        "s_waitcnt lgkmcnt(3)\n\t" "v_mfma_f32_32x32x16_bf16 %0, %8, %3, %0\n\t"
        "v_xor_b32 %5, (((%c19+6)&7)<<5), %18\n\t" "ds_read_b128 %3, %5 offset:((%c19+6)>>3)*256\n\t"
        "s_waitcnt lgkmcnt(3)\n\t" "v_mfma_f32_32x32x16_bf16 %0, %9, %4, %0\n\t"
        "v_xor_b32 %5, (((%c19+7)&7)<<5), %18\n\t" "ds_read_b128 %4, %5 offset:((%c19+7)>>3)*256\n\t"
        "s_waitcnt lgkmcnt(3)\n\t" "v_mfma_f32_32x32x16_bf16 %0, %10, %1, %0\n\t"
        "v_xor_b32 %5, (((%c19+8)&7)<<5), %18\n\t" "ds_read_b128 %1, %5 offset:((%c19+8)>>3)*256\n\t"
        "s_waitcnt lgkmcnt(3)\n\t" "v_mfma_f32_32x32x16_bf16 %0, %11, %2, %0\n\t"
        "v_xor_b32 %5, (((%c19+9)&7)<<5), %18\n\t" "ds_read_b128 %2, %5 offset:((%c19+9)>>3)*256\n\t"
        "s_waitcnt lgkmcnt(3)\n\t" "v_mfma_f32_32x32x16_bf16 %0, %12, %3, %0\n\t"
        "v_xor_b32 %5, (((%c19+10)&7)<<5), %18\n\t" "ds_read_b128 %3, %5 offset:((%c19+10)>>3)*256\n\t"
        "s_waitcnt lgkmcnt(3)\n\t" "v_mfma_f32_32x32x16_bf16 %0, %13, %4, %0\n\t"
        "v_xor_b32 %5, (((%c19+11)&7)<<5), %18\n\t" "ds_read_b128 %4, %5 offset:((%c19+11)>>3)*256\n\t"
        "s_waitcnt lgkmcnt(3)\n\t" "v_mfma_f32_32x32x16_bf16 %0, %14, %1, %0\n\t"
        "s_waitcnt lgkmcnt(2)\n\t" "v_mfma_f32_32x32x16_bf16 %0, %15, %2, %0\n\t"
        "s_waitcnt lgkmcnt(1)\n\t" "v_mfma_f32_32x32x16_bf16 %0, %16, %3, %0\n\t"
        "s_waitcnt lgkmcnt(0)\n\t" "v_mfma_f32_32x32x16_bf16 %0, %17, %4, %0\n\t"
        "s_nop 15\n\t"
        : "+v"(acc), "=&v"(t0), "=&v"(t1), "=&v"(t2), "=&v"(t3), "=&v"(a)
        : "v"(wf[0]), "v"(wf[1]), "v"(wf[2]), "v"(wf[3]), "v"(wf[4]), "v"(wf[5]), "v"(wf[6]), "v"(wf[7]), "v"(wf[8]), "v"(wf[9]), "v"(wf[10]), "v"(wf[11]),
          "v"(ob), "i"(I0)
        : "memory");
}

}

#define KVSLEEP 8
#define POLLSLEEP 4

extern "C" __global__ __launch_bounds__(768) void gm_fused(const u16* __restrict__ x, const u16* __restrict__ kin, const u16* __restrict__ vin,
                                                           const u16* __restrict__ shiftk, const u16* __restrict__ ssmk, const u16* __restrict__ ssmD,
                                                           const u16* __restrict__ wq, const u16* __restrict__ wk, const u16* __restrict__ wv,
                                                           const u16* __restrict__ wo, const u16* __restrict__ gw, char* __restrict__ ws,
                                                           u16* __restrict__ out) {
    __shared__ __attribute__((aligned(16))) char smem[K2_LDS];
    constexpr unsigned CNT = K2_CNT;
    const char LDSP* sm = (const char LDSP*)smem;
    char LDSP* smw = (char LDSP*)smem;
    const unsigned lds0 = (unsigned)(size_t)sm;
    const int lane = threadIdx.x & 63;
    const int w = __builtin_amdgcn_readfirstlane(threadIdx.x >> 6);
    const int bid = blockIdx.x;
    asm volatile("" :: "s"(x), "s"(kin), "s"(vin), "s"(shiftk), "s"(ssmk), "s"(ssmD), "s"(wq), "s"(wk), "s"(wv), "s"(wo), "s"(gw), "s"(ws), "s"(out));
    const rsrc_t rsWS = mk_rsrc(ws, WS_END);
    const int tt = (bid & 7) * 4 + (bid >> 6), j = (bid >> 3) & 7;
    const int tb = (bid & 7) * 8 + (bid >> 5), hg = (bid >> 3) & 3;
    const int r = lane & 31, h = lane >> 5;
    STAMP_DECL;
    STAMP(0);
    u32x2 ep;
    bload8_sc1(ep, rsWS, WS_EP);
    if (w == 0) { if (lane < 32) *(unsigned LDSP*)(smw + CNT + lane * 4) = 0; wait_lgkm0(); }
    barrier();

    if (w >= 8) {
        const int lw = w - 8;
        __builtin_amdgcn_s_setprio(3);
        const rsrc_t rsX = mk_rsrc(x, S_LEN * DM * 2), rsW = mk_rsrc(wq, DM * DM * 2);
        const unsigned gr = lane >> 3, gc = lane & 7;
        const unsigned row0 = 16 * lw + gr;
        const unsigned vx0 = (tt * 64 + row0) * 1024 + ((gc ^ ((row0 >> 1) & 7)) << 4);
        const unsigned vx1 = (tt * 64 + row0 + 8) * 1024 + ((gc ^ (((row0 + 8) >> 1) & 7)) << 4);
        const unsigned vw0 = row0 * 1024 + j * 128 + ((gc ^ (((gr >> 1) & 1) << 2)) << 4);
        auto chunk = [&](int kc) {
            dma1(rsX, vx0, kc * 128, lds0 + G_XS + kc * 8192 + lw * 2048);
            dma1(rsX, vx1, kc * 128, lds0 + G_XS + kc * 8192 + lw * 2048 + 1024);
            dma1(rsW, vw0, kc * 65536, lds0 + G_WS + kc * 8192 + lw * 2048);
            dma1(rsW, vw0, kc * 65536 + 8192, lds0 + G_WS + kc * 8192 + lw * 2048 + 1024);
        };
        if (j < 2) {
            const rsrc_t rsE = mk_rsrc(j == 0 ? wk : wv, DM * 16 * 2);
#pragma unroll
            for (int i = 0; i < 4; ++i) {
                const unsigned prow = (lw * 4 + i) * 32 + (lane >> 1);
                const unsigned grow = prow ^ (((prow >> 3) & 1) << 2);
                dma1(rsE, grow * 32 + (lane & 1) * 16, 0, lds0 + K1_WE + (lw * 4 + i) * 1024);
            }
        } else if (j == 2) {
            if (lw == 0) dma1(mk_rsrc(gw, DM * 2), lane * 16, 0, lds0 + K1_WE);
        }
        if (lw >= 2)
            dma1(mk_rsrc(wo, DM * DM * 2), (32 * (bid & 15) + 16 * (lw - 2) + (lane >> 2)) * 1024 + (bid >> 4) * 64 + (lane & 3) * 16, 0,
                 lds0 + K1_WO + (lw - 2) * 1024);
        chunk(0); chunk(1); chunk(2); chunk(3);
        chunk(4); chunk(5);
        wait_vm<16>(); lds_post(smw + CNT + 4 * D_S0, lane);
        chunk(6); chunk(7);
        wait_vm<16>(); lds_post(smw + CNT + 4 * D_S1, lane);
        wait_vm<8>();  lds_post(smw + CNT + 4 * D_S2, lane);
        wait_vm<4>();  lds_post(smw + CNT + 4 * D_S3, lane);
        wait_vm<0>();  lds_post(smw + CNT + 4 * D_ALL, lane);
        STAMP(1);
        lds_wait(sm + CNT + 4 * D_ALL, 4);
        if (lw >= 2) {
            const unsigned G = lane >> 4, q = (lane & 15) >> 2, p = lane & 3;
            const unsigned a = K1_WO + (16 * (lw - 2) + 8 * (G >> 1) + q) * 64 + (16 * (G & 1) + 4 * p) * 2;
            st16_sc1(rsWS, WS_WO + ((bid >> 4) * 32 + 2 * (bid & 15) + (lw - 2)) * 1024 + lane * 16, cat2(ldstr(sm + a), ldstr(sm + a + 256)));
        }
        if (j < 3) {
            const unsigned row = 16 * lw + (lane & 15), g = lane >> 4;
            const unsigned ex0 = G_XS + row * 128 + ((g ^ ((row >> 1) & 7)) << 4), ex1 = G_XS + row * 128 + (((4 + g) ^ ((row >> 1) & 7)) << 4);
            const unsigned q = (lane & 15) >> 2, p = lane & 3, n = lane & 15;
            const unsigned ee0 = K1_WE + (8 * g + q + 4 * (0 ^ (g & 1))) * 32 + 8 * p, ee1 = K1_WE + (8 * g + q + 4 * (1 ^ (g & 1))) * 32 + 8 * p;
            f32x4 e0 = {}, e1 = {};
            if (j == 0) {
#pragma unroll
                for (int kc = 0; kc < 8; ++kc) {
                    e0 = mfma16(cat2(ldstr(sm + ee0 + kc * 2048), ldstr(sm + ee1 + kc * 2048)), lds16(sm + ex0 + kc * 8192), e0);
                    e1 = mfma16(cat2(ldstr(sm + ee0 + kc * 2048 + 1024), ldstr(sm + ee1 + kc * 2048 + 1024)), lds16(sm + ex1 + kc * 8192), e1);
                }
                const unsigned p0 = pack2(e0[0] + e1[0], e0[1] + e1[1]), p1 = pack2(e0[2] + e1[2], e0[3] + e1[3]);
                const int key = tt * 64 + 16 * lw + n;
                st8_sc1(rsWS, WS_KV + (key >> 5) * 1024 + (g >> 1) * 512 + (key & 31) * 16 + (g & 1) * 8, u32x2{p0, p1});
                float ss = dot2(p0, p0, dot2(p1, p1, 0.f));
                ss += __shfl_xor(ss, 16); ss += __shfl_xor(ss, 32);
                const unsigned m = wave_umax(__builtin_bit_cast(unsigned, ss));
                if (lane == 0) st4_sc1(rsWS, WS_KN + (tt * 4 + lw) * 4, m);
            } else if (j == 1) {
#pragma unroll
                for (int kc = 0; kc < 8; ++kc) {
                    e0 = mfma16(lds16(sm + ex0 + kc * 8192), cat2(ldstr(sm + ee0 + kc * 2048), ldstr(sm + ee1 + kc * 2048)), e0);
                    e1 = mfma16(lds16(sm + ex1 + kc * 8192), cat2(ldstr(sm + ee0 + kc * 2048 + 1024), ldstr(sm + ee1 + kc * 2048 + 1024)), e1);
                }
                const unsigned p0 = pack2(e0[0] + e1[0], e0[1] + e1[1]), p1 = pack2(e0[2] + e1[2], e0[3] + e1[3]);
                const int tile = 2 * tt + (lw >> 1), pp = lw & 1;
                const unsigned vb = WS_KV + KF_BYTES + tile * 1088 + pp * 544;
                st8_sc1(rsWS, vb + ((g >> 1) * 16 + n) * 16 + (g & 1) * 8, u32x2{p0, p1});
                if (lane < 2) {
                    const unsigned f = lane == 0 ? 0x3f803f80u : 0u;
                    st16_sc1(rsWS, vb + (32 + lane) * 16, u32x4{f, f, f, f});
                }
                const unsigned a0 = p0 & 0x7fff7fffu, a1 = p1 & 0x7fff7fffu;
                const unsigned m = wave_umax(max(max(a0 & 0xffffu, a0 >> 16), max(a1 & 0xffffu, a1 >> 16)));
                if (lane == 0) st4_sc1(rsWS, WS_KN + (128 + tt * 4 + lw) * 4, m << 16);
            } else {
#pragma unroll
                for (int kc = 0; kc < 8; ++kc) {
                    e0 = mfma16(lds16(sm + ex0 + kc * 8192), lds16(sm + K1_WE + g * 16 + kc * 128), e0);
                    e1 = mfma16(lds16(sm + ex1 + kc * 8192), lds16(sm + K1_WE + g * 16 + kc * 128 + 64), e1);
                }
                if (n == 0) {
                    u32x4 gv;
#pragma unroll
                    for (int r = 0; r < 4; ++r) gv[r] = __builtin_bit_cast(unsigned, rbf(1.f / (1.f + __expf(-rbf(e0[r] + e1[r])))));
                    st16_sc1(rsWS, WS_G + (tt * 64 + 16 * lw + 4 * g) * 4, gv);
                }
            }
        }
        wait_lgkm0();
        lds_post(smw + CNT + 4 * D_LD, lane);
        asm volatile("s_waitcnt vmcnt(0)" : "+v"(ep) :: "memory");
        STAMP(3);
    } else if (w >= 4) {
        const int kw = w - 4;
        __builtin_amdgcn_s_sleep(KVSLEEP);
        const rsrc_t rsV = mk_rsrc(vin, S_LEN * DM * 2), rsS = mk_rsrc(shiftk, DM * 4 * 2), rsK = mk_rsrc(kin, S_LEN * DM * 2);
        const int oct = lane & 7, t0 = 16 * kw + 2 * (lane >> 3);
        u32x4 va, vb, sk0, sk1, sk2, sk3;
        u32x4 R0, R1, R2, R3, R4;
        {
            const unsigned vk = (unsigned)((tt * 64 - 3 + t0) * 1024 + j * 128 + oct * 16);
            bload16(R0, rsK, vk); bload16(R1, rsK, vk + 1024); bload16(R2, rsK, vk + 2048); bload16(R3, rsK, vk + 3072); bload16(R4, rsK, vk + 4096);
        }
        bload16(va, rsV, (tt * 64 + t0) * 1024 + j * 128 + oct * 16);
        bload16(vb, rsV, (tt * 64 + t0 + 1) * 1024 + j * 128 + oct * 16);
        {
            const unsigned so = (j * 64 + oct * 8) * 8;
            bload16(sk0, rsS, so); bload16(sk1, rsS, so + 16); bload16(sk2, rsS, so + 32); bload16(sk3, rsS, so + 48);
        }
        asm volatile("s_waitcnt vmcnt(0)" : "+v"(va), "+v"(vb), "+v"(sk0), "+v"(sk1), "+v"(sk2), "+v"(sk3), "+v"(R0), "+v"(R1), "+v"(R2), "+v"(R3), "+v"(R4),
                     "+v"(ep) :: "memory");
        STAMP(1);
#pragma unroll
        for (int i = 0; i < 2; ++i)
            st16_sc1(rsWS, WS_O + (tb * 32 + 8 * kw + 4 * i + (lane >> 4)) * 1024 + hg * 256 + (lane & 15) * 16, u32x4{POISON, POISON, POISON, POISON});
        {
            const u32x4 R[5] = {R0, R1, R2, R3, R4};
            const u32x4 sk[4] = {sk0, sk1, sk2, sk3};
            unsigned tw[8];
#pragma unroll
            for (int c = 0; c < 8; ++c) {
                const int d = c >> 1;
                const unsigned sel = (c & 1) ? 0x07060302u : 0x05040100u;
                const unsigned s01 = sk[d][(c & 1) * 2], s23 = sk[d][(c & 1) * 2 + 1];
                const float a0 = dot2(__builtin_amdgcn_perm(R[2][d], R[3][d], sel), s01, dot2(__builtin_amdgcn_perm(R[0][d], R[1][d], sel), s23, 0.f));
                const float a1 = dot2(__builtin_amdgcn_perm(R[3][d], R[4][d], sel), s01, dot2(__builtin_amdgcn_perm(R[1][d], R[2][d], sel), s23, 0.f));
                const float v0 = (c & 1) ? bfhi(va[d]) : bflo(va[d]), v1 = (c & 1) ? bfhi(vb[d]) : bflo(vb[d]);
                tw[c] = pack2(a1 * v1, a0 * v0);
            }
            const unsigned mp = 62 - t0;
#pragma unroll
            for (int c = 0; c < 8; ++c)
                *(unsigned LDSP*)(smw + K1_KT + (8 * oct + c) * 128 + (((mp >> 3) ^ oct) << 4) + (mp & 7) * 2) = tw[c];
            wait_lgkm0();
            lds_post(smw + CNT + 4 * D_TS, lane);
        }
        lds_wait(sm + CNT + 4 * D_TS, 4);
#pragma unroll
        for (int i = 0; i < 2; ++i) {
            const unsigned ch = 16 * kw + 8 * i + (lane >> 3), c = lane & 7;
            st16_sc1(rsWS, WS_UR + (j * 64 + ch) * 4096 + (1984 - 64 * tt + 8 * c) * 2, lds16(sm + K1_KT + ch * 128 + ((c ^ (ch >> 3)) << 4)));
        }
        lds_post(smw + CNT + 4 * D_TSF, lane);
        STAMP(2);
        wait_vm<0>();
        STAMP(3);
    } else {
        const int wm = w & 1, wn = w >> 1;
        const GemmAddr ga = gemm_addr(wm, wn, lane);
        f32x16 acc = {};
        lds_wait(sm + CNT + 4 * D_S0, 4); STAMP(1); gemm_stage(sm, ga, 0, acc);
        lds_wait(sm + CNT + 4 * D_S1, 4); gemm_stage(sm, ga, 2, acc);
        lds_wait(sm + CNT + 4 * D_S2, 4); gemm_stage(sm, ga, 4, acc);
        lds_wait(sm + CNT + 4 * D_S3, 4); gemm_chunk(sm, ga, 6, acc);
        lds_wait(sm + CNT + 4 * D_ALL, 4); gemm_chunk(sm, ga, 7, acc);
        asm volatile("" : "+v"(acc));
        lds_post(smw + CNT + 4 * D_LD, lane);
        STAMP(2);
        u32x2 p[4];
#pragma unroll
        for (int q = 0; q < 4; ++q) {
            const unsigned a = pack2(acc[4 * q], acc[4 * q + 1]), b = pack2(acc[4 * q + 2], acc[4 * q + 3]);
            p[q][0] = pack2(bflo(a) * QSCALE, bfhi(a) * QSCALE);
            p[q][1] = pack2(bflo(b) * QSCALE, bfhi(b) * QSCALE);
        }
        u32x4 r0, r1;
        quads_to_rows(p, r0, r1);
        lds_wait(sm + CNT + 4 * D_TSF, 4);
        {
            const unsigned row = 32 * wm + r, c0 = 4 * wn + h;
            *(u32x4 LDSP*)(smw + K1_KT + row * 128 + ((c0 ^ (row & 7)) << 4)) = r0;
            *(u32x4 LDSP*)(smw + K1_KT + row * 128 + (((c0 + 2) ^ (row & 7)) << 4)) = r1;
        }
        wait_lgkm0();
        lds_post(smw + CNT + 4 * D_QS, lane);
        lds_wait(sm + CNT + 4 * D_QS, 4);
#pragma unroll
        for (int i = 0; i < 2; ++i) {
            const unsigned row = 16 * w + 8 * i + (lane >> 3), c = lane & 7;
            st16_sc1(rsWS, WS_Q + (tt * 64 + row) * 1024 + j * 128 + c * 16, lds16(sm + K1_KT + row * 128 + ((c ^ (row & 7)) << 4)));
        }
        asm volatile("s_waitcnt vmcnt(0)" : "+v"(ep) :: "memory");
        STAMP(3);
    }
    const unsigned long long tag = ((unsigned long long)(unsigned)__builtin_amdgcn_readfirstlane((int)ep[1]) << 32 |
                                    (unsigned)__builtin_amdgcn_readfirstlane((int)ep[0])) + 1;
    const unsigned tlo = (unsigned)tag, thi = (unsigned)(tag >> 32);
    if (lds_inc(smw + CNT + 4 * D_P1, lane) == 11) {
        if (lane < NREP) st8_sc1(rsWS, WS_F1 + lane * 2048 + bid * 8, u32x2{tlo, thi});
    }
    STAMP(4);

    if (w < 8) {
        const int rg = w & 3, kh = w >> 2;
        u32x4 q0, q1;
        const unsigned qoff = WS_Q + (unsigned)(tb * 32 + 8 * rg + (r >> 3)) * 1024 + (8 * hg + (r & 7)) * 32 + h * 16;
        const unsigned kaddr = K2_K + kh * 32 * 1024 + lane * 16;
        const unsigned vaddr = K2_V + kh * 32 * 1088 + (r < 16 ? (h * 16 + r) * 16 : ((r & 11) == 0 ? 32 * 16 : 33 * 16));
        if (w == 0) {
            const unsigned fo = WS_F1 + (bid & (NREP - 1)) * 2048 + (((tb >> 3)) + 8 * (2 * hg + (lane & 1)) + 64 * ((tb >> 1) & 3)) * 8;
            for (unsigned spins = 0; spins < (1u << 20); ++spins) {
                u32x2 f;
                bload8_sc1(f, rsWS, fo);
                asm volatile("s_waitcnt vmcnt(0)" : "+v"(f) :: "memory");
                if (__all(f[0] == tlo && f[1] == thi)) break;
                __builtin_amdgcn_s_sleep(POLLSLEEP);
            }
            lds_post(smw + CNT + 4 * C_QOK, lane);
        } else {
            lds_wait(sm + CNT + 4 * C_QOK, 1);
        }
        bload16_sc1(q0, rsWS, qoff + kh * 4096);
        bload16_sc1(q1, rsWS, qoff + (kh ^ 1) * 4096);
        asm volatile("s_waitcnt vmcnt(0)" : "+v"(q0), "+v"(q1) :: "memory");
        f32x16 O0 = {}, O1 = {};
        STAMP(9);
        attn_fast(O0, O1, q0, q1, lds0 + kaddr, lds0 + vaddr, lds0 + CNT);
        STAMP(10);
        barrier();
        float m0 = 0.f, m1 = 0.f;
        const u32x4 slow4 = lds16(sm + CNT + 4 * C_SLOW);
        const bool anyslow = __builtin_amdgcn_readfirstlane((int)(slow4[0] | slow4[1] | slow4[2] | slow4[3])) != 0;
        bool slow = false;
        if (anyslow) {
            slow = __builtin_amdgcn_readfirstlane((int)lds_ld(sm + CNT + 4 * (C_SLOW + rg))) != 0;
            if (slow) {
                O0 = f32x16{}; O1 = f32x16{};
                m0 = m1 = -__builtin_inff();
                attn_slow(O0, m0, q0, sm, kaddr, vaddr);
                attn_slow(O1, m1, q1, sm, kaddr, vaddr);
            }
            barrier();
        }
        STAMP(11);
        {
            char LDSP* pw = smw + K2_PART + w * 2560;
            *(f32x4 LDSP*)(pw + lane * 16) = f32x4{O1[0], O1[1], O1[2], O1[3]};
            *(f32x4 LDSP*)(pw + 1024 + lane * 16) = f32x4{O1[4], O1[5], O1[6], O1[7]};
            *(f32x2 LDSP*)(pw + 2048 + lane * 8) = f32x2{O1[8], m1};
        }
        wait_lgkm0();
        barrier();
        STAMP(12);
        u32x4 o;
        {
            const char LDSP* pr = sm + K2_PART + (w ^ 4) * 2560;
            f32x4 pa = *(const f32x4 LDSP*)(pr + lane * 16), pb = *(const f32x4 LDSP*)(pr + 1024 + lane * 16);
            const f32x2 lm = *(const f32x2 LDSP*)(pr + 2048 + lane * 8);
            f32x4 ka = f32x4{O0[0], O0[1], O0[2], O0[3]}, kb = f32x4{O0[4], O0[5], O0[6], O0[7]};
            float l0 = O0[8], l1 = lm[0];
            if (slow) {
                const float mn = fmaxf(m0, lm[1]);
                const float s0 = __builtin_amdgcn_exp2f(m0 - mn), s1 = __builtin_amdgcn_exp2f(lm[1] - mn);
                ka *= s0; kb *= s0; l0 *= s0; pa *= s1; pb *= s1; l1 *= s1;
            }
            const float inv = __builtin_amdgcn_rcpf(l0 + l1);
            ka = (ka + pa) * inv; kb = (kb + pb) * inv;
            auto s0 = __builtin_amdgcn_permlane32_swap(pack2(ka[0], ka[1]), pack2(kb[0], kb[1]), false, false);
            auto s1 = __builtin_amdgcn_permlane32_swap(pack2(ka[2], ka[3]), pack2(kb[2], kb[3]), false, false);
            o = u32x4{min(s0[0], POISON - 1), min(s1[0], POISON - 1), min(s0[1], POISON - 1), min(s1[1], POISON - 1)};
        }
        const int tokl = 8 * rg + 4 * kh + (r >> 3);
        st16_sc1(rsWS, WS_O + (tb * 32 + tokl) * 1024 + (8 * hg + (r & 7)) * 32 + h * 16, o);
        *(u32x4 LDSP*)(smw + K2_OI + tokl * 1024 + ((((r & 7) * 2 + h) ^ (tokl & 15)) << 4)) = o;
        const int tk = 4 * w + (lane >> 4), c = lane & 15;
        const unsigned poff = WS_O + (tb * 32 + tk) * 1024 + c * 16;
        const unsigned po0 = poff + ((hg + 1) & 3) * 256, po1 = poff + ((hg + 2) & 3) * 256, po2 = poff + ((hg + 3) & 3) * 256;
        wait_lgkm0();
        barrier();
        STAMP(13);
        __builtin_amdgcn_s_sleep(XSLEEP);
        u32x4 p0, p1, p2;
        bload16_sc1(p0, rsWS, po0); bload16_sc1(p1, rsWS, po1); bload16_sc1(p2, rsWS, po2);
        asm volatile("s_waitcnt vmcnt(0)" : "+v"(p0), "+v"(p1), "+v"(p2) :: "memory");
        for (unsigned spins = 0; spins < (1u << 20); ++spins) {
            const unsigned mx = max(max(max(max(p0[0], p0[1]), max(p0[2], p0[3])), max(max(p1[0], p1[1]), max(p1[2], p1[3]))),
                                    max(max(p2[0], p2[1]), max(p2[2], p2[3])));
            if (__all(mx != POISON)) break;
            bload16_sc1(p0, rsWS, po0); bload16_sc1(p1, rsWS, po1); bload16_sc1(p2, rsWS, po2);
            asm volatile("s_waitcnt vmcnt(0)" : "+v"(p0), "+v"(p1), "+v"(p2) :: "memory");
        }
        *(u32x4 LDSP*)(smw + K2_OI + tk * 1024 + (((16 + c) ^ (tk & 15)) << 4)) = p0;
        *(u32x4 LDSP*)(smw + K2_OI + tk * 1024 + (((32 + c) ^ (tk & 15)) << 4)) = p1;
        *(u32x4 LDSP*)(smw + K2_OI + tk * 1024 + (((48 + c) ^ (tk & 15)) << 4)) = p2;
        STAMP(14);
        wait_lgkm0();
        barrier();
        STAMP(15);
    } else {
        const int hw = w - 8;
        __builtin_amdgcn_s_setprio(3);
        const int half = hw & 1, isv = hw >> 1, rot = (bid >> 5) & 7;
        lds_wait(sm + CNT + 4 * D_LD, 8);
        {
            const int ptt = 16 * half + (lane & 15);
            const unsigned fo = WS_F1 + (bid & (NREP - 1)) * 2048 + ((ptt >> 2) + 8 * isv + 64 * (ptt & 3)) * 8;
            unsigned long long ready = 0;
            const unsigned src0 = isv ? WS_KV + K2_V + half * 34816 : WS_KV + half * 32768, dst0 = lds0 + (isv ? K2_V + half * 34816 : half * 32768);
            const unsigned gb = isv ? 4352 : 4096;
#pragma unroll
            for (int g = 0; g < 8; ++g) {
                const int G = (g + rot) & 7;
                for (unsigned spins = 0; ((ready >> (2 * G)) & 3) != 3 && spins < (1u << 20); ++spins) {
                    u32x2 f;
                    bload8_sc1(f, rsWS, fo);
                    asm volatile("s_waitcnt vmcnt(0)" : "+v"(f) :: "memory");
                    ready = __ballot(f[0] == tlo && f[1] == thi);
                    if (((ready >> (2 * G)) & 3) != 3) __builtin_amdgcn_s_sleep(POLLSLEEP);
                }
                if (g == 0) STAMP(8);
                dma4_sc1(rsWS, lane * 16, src0 + G * gb, dst0 + g * gb);
                if (isv) { if (lane < 16) dma1_sc1(rsWS, lane * 16, src0 + G * gb + 4096, dst0 + g * gb + 4096); }
                if (g >= 1 && g <= 4) {
                    if (isv) wait_vm<5>(); else wait_vm<4>();
                    lds_post(smw + CNT + 4 * (C_ST0 + g - 1), lane);
                }
            }
        }
        lds_wait(sm + CNT + 4 * C_QOK, 1);
        u32x4 q0, q1;
        {
            const unsigned qoff = WS_Q + (unsigned)(tb * 32 + 8 * hw + (r >> 3)) * 1024 + (8 * hg + (r & 7)) * 32 + h * 16;
            bload16_sc1(q0, rsWS, qoff); bload16_sc1(q1, rsWS, qoff + 4096);
        }
        wait_vm<2>(); lds_post(smw + CNT + 4 * C_ST4, lane);
        if (hw == 0) {
            const unsigned fo = WS_F1 + (bid & (NREP - 1)) * 2048 + lane * 32;
            for (unsigned spins = 0; spins < (1u << 20); ++spins) {
                u32x4 f0, f1;
                bload16_sc1(f0, rsWS, fo); bload16_sc1(f1, rsWS, fo + 16);
                asm volatile("s_waitcnt vmcnt(0)" : "+v"(f0), "+v"(f1) :: "memory");
                const unsigned dlo = (f0[0] ^ tlo) | (f0[2] ^ tlo) | (f1[0] ^ tlo) | (f1[2] ^ tlo);
                const unsigned dhi = (f0[1] ^ thi) | (f0[3] ^ thi) | (f1[1] ^ thi) | (f1[3] ^ thi);
                if (__all((dlo | dhi) == 0)) break;
                __builtin_amdgcn_s_sleep(16);
            }
            lds_post(smw + CNT + 4 * C_GOB, lane);
        } else {
            lds_wait(sm + CNT + 4 * C_GOB, 1);
        }
        const int ch = hw >> 1, st = hw & 1;
        dma4(mk_rsrc(ssmk, DM * S_LEN * 2), st * 2 + lane * 16, (bid * 2 + ch) * 4096, lds0 + K2_H + (ch * 2 + st) * K2_HC + 32);
        dma2_sc1(rsWS, lane * 16, WS_UR + (bid * 2 + ch) * 4096 + st * 2048, lds0 + K2_U + ch * K2_UC + st * 2048);
        u32x2 kn, vm;
        bload8_sc1(kn, rsWS, WS_KN + lane * 8); bload8_sc1(vm, rsWS, WS_KN + 512 + lane * 8);
        wait_vm<2>();  lds_post(smw + CNT + 4 * (C_CH0 + ch), lane);
        asm volatile("s_waitcnt vmcnt(0)" : "+v"(q0), "+v"(q1), "+v"(kn), "+v"(vm) :: "memory");
        {
            const float kk = u2f(wave_umax(max(kn[0], kn[1]))), vx = u2f(wave_umax(max(vm[0], vm[1])));
            float qq0 = 0.f, qq1 = 0.f;
#pragma unroll
            for (int i = 0; i < 4; ++i) { qq0 = dot2(q0[i], q0[i], qq0); qq1 = dot2(q1[i], q1[i], qq1); }
            float qq = fmaxf(qq0 + __shfl_xor(qq0, 32), qq1 + __shfl_xor(qq1, 32));
            if (!(qq0 == qq0) || !(qq1 == qq1)) qq = __builtin_inff();
            const bool fast = __all(qq * kk <= 3600.f && vx <= 1.0e12f);
            if (lane == 0) *(unsigned LDSP*)(smw + CNT + 4 * (C_SLOW + hw)) = fast ? 0u : 1u;
        }
        lds_wait(sm + CNT + 4 * (C_CH0 + ch), 2);
        __builtin_amdgcn_s_setprio(2);
        STAMP(9);
        {
            const u16 h0 = *(const u16 LDSP*)(sm + K2_H + (ch * 2) * K2_HC + 32);
            if (lane < 16) {
                *(u16 LDSP*)(smw + K2_H + (ch * 2) * K2_HC + lane * 2) = 0;
                *(u16 LDSP*)(smw + K2_H + (ch * 2 + 1) * K2_HC + lane * 2) = lane == 15 ? h0 : (u16)0;
            }
            if (lane < 32) *(u32x4 LDSP*)(smw + K2_U + ch * K2_UC + 4096 + lane * 16) = u32x4{0, 0, 0, 0};
            wait_lgkm0();
            const float dskip = bflo((unsigned)ssmD[bid * 2 + ch]);
            const unsigned yoff = WS_YT + (bid * 2 + ch) * 4096;
            if (st) conv_wave<1>(sm, ch, lane, dskip, rsWS, yoff); else conv_wave<0>(sm, ch, lane, dskip, rsWS, yoff);
        }
        STAMP(10);
        if (bid == 0 && hw == 0 && lane == 0) st8_sc1(rsWS, WS_EP, u32x2{tlo, thi});
        wait_vm<0>();
        if (lds_inc(smw + CNT + 4 * C_YST, lane) == 3) {
            if (lane == 0) st8_sc1(rsWS, WS_FY + bid * 8, u32x2{tlo, thi});
        }
        lds_wait(sm + CNT + 4 * C_YST, 4);
        const int nt = 4 * hg + hw;
        {
            const rsrc_t rsX = mk_rsrc(x, S_LEN * DM * 2);
            const unsigned vxo = (tb * 32 + (lane >> 2)) * 1024 + nt * 64 + (lane & 3) * 16;
            dma1(rsX, vxo, 0, lds0 + K2_XP + hw * 2048);
            dma1(rsX, vxo, 16 * 1024, lds0 + K2_XP + hw * 2048 + 1024);
        }
        const float g = u2f(__builtin_amdgcn_raw_buffer_load_b32(rsWS, WS_G + (tb * 32 + r) * 4, 0, 16));
        u32x4 wf[32];
#pragma unroll
        for (int i = 0; i < 32; ++i) wf[i] = ld16_sc1(rsWS, WS_WO + (nt * 32 + ((8 * hg + i) & 31)) * 1024 + lane * 16);
        {
            for (unsigned spins = 0; spins < (1u << 20); ++spins) {
                u32x2 f;
                bload8_sc1(f, rsWS, WS_FY + (16 * nt + (lane & 15)) * 8);
                asm volatile("s_waitcnt vmcnt(0)" : "+v"(f) :: "memory");
                if (__all(f[0] == tlo && f[1] == thi)) break;
                __builtin_amdgcn_s_sleep(32);
            }
#pragma unroll
            for (int i = 0; i < 2; ++i) {
                const u32x4 yv = ld16_sc1(rsWS, WS_YT + (nt * 32 + 16 * i + (lane >> 2)) * 4096 + tb * 64 + (lane & 3) * 16);
                *(u32x4 LDSP*)(smw + K2_YP + hw * 2048 + i * 1024 + lane * 16) = yv;
            }
        }
        wait_vm<0>();
        wait_lgkm0();
        u32x2 xy[4];
        {
            const unsigned G = lane >> 4, qq = (lane & 15) >> 2, pp = lane & 3;
#pragma unroll
            for (int q = 0; q < 4; ++q) {
                const u32x2 y = ldstr(sm + K2_YP + hw * 2048 + (8 * q + 4 * (G >> 1) + qq) * 64 + (16 * (G & 1) + 4 * pp) * 2);
                const u32x2 xx = *(const u32x2 LDSP*)(sm + K2_XP + hw * 2048 + r * 64 + (8 * q + 4 * h) * 2);
                xy[q] = u32x2{pack2(bflo(xx[0]) * bflo(y[0]) * g, bfhi(xx[0]) * bfhi(y[0]) * g), pack2(bflo(xx[1]) * bflo(y[1]) * g, bfhi(xx[1]) * bfhi(y[1]) * g)};
            }
        }
        float g1 = 1.f - g;
        asm volatile("" : "+v"(xy[0]), "+v"(xy[1]), "+v"(xy[2]), "+v"(xy[3]), "+v"(g1));
        STAMP(11);
        barrier();
        {
            const u32x4 slow4 = lds16(sm + CNT + 4 * C_SLOW);
            if (__builtin_amdgcn_readfirstlane((int)(slow4[0] | slow4[1] | slow4[2] | slow4[3])) != 0) barrier();
        }
        barrier();
        barrier();
        f32x16 acc = {};
#pragma unroll
        for (int i = 0; i < 8; ++i) acc = mfma32(wf[i], lds16(sm + K2_OI + r * 1024 + (((2 * i + h) ^ (r & 15)) << 4)), acc);
        const unsigned ob = lds0 + K2_OI + r * 1024 + ((h ^ (r & 15)) << 4);
        STAMP(12);
        barrier();
        STAMP(13);
        oproj12<8>(acc, wf + 8, ob);
        oproj12<20>(acc, wf + 20, ob);
        STAMP(14);
        u32x2 p[4];
#pragma unroll
        for (int q = 0; q < 4; ++q) {
            p[q][0] = pack2(fmaf(g1, acc[4 * q], bflo(xy[q][0])), fmaf(g1, acc[4 * q + 1], bfhi(xy[q][0])));
            p[q][1] = pack2(fmaf(g1, acc[4 * q + 2], bflo(xy[q][1])), fmaf(g1, acc[4 * q + 3], bfhi(xy[q][1])));
        }
        u32x4 b0, b1;
        quads_to_rows(p, b0, b1);
        char* op = (char*)out + (size_t)(tb * 32 + r) * 1024 + nt * 64 + h * 16;
        *(u32x4*)op = b0;
        *(u32x4*)(op + 32) = b1;
        STAMP(15);
    }
    STAMP_FLUSH();
}

extern "C" void kernel_launch(void* const* d_in, const int* in_sizes, int n_in, void* d_out, int out_size, void* d_ws, size_t ws_size,
                              hipStream_t stream) {
    gm_fused<<<256, 768, 0, stream>>>((const u16*)d_in[0], (const u16*)d_in[1], (const u16*)d_in[2], (const u16*)d_in[3], (const u16*)d_in[4],
                                      (const u16*)d_in[5], (const u16*)d_in[7], (const u16*)d_in[8], (const u16*)d_in[9], (const u16*)d_in[10],
                                      (const u16*)d_in[11], (char*)d_ws, (u16*)d_out);
}
```

```cpp
#include <hip/hip_runtime.h>
#include <hip/hip_bf16.h>
#include <stdint.h>

namespace {

typedef unsigned short u16;
typedef __attribute__((ext_vector_type(8))) __bf16 bf16x8;
typedef __attribute__((ext_vector_type(16))) __bf16 bf16x16;
typedef __attribute__((ext_vector_type(2))) __bf16 bf16x2;
typedef __attribute__((ext_vector_type(4))) short s16x4;
typedef __attribute__((ext_vector_type(2))) float f32x2;
typedef __attribute__((ext_vector_type(4))) float f32x4;
typedef __attribute__((ext_vector_type(8))) float f32x8;
typedef __attribute__((ext_vector_type(16))) float f32x16;
typedef __attribute__((ext_vector_type(2))) unsigned u32x2;
typedef __attribute__((ext_vector_type(4))) unsigned u32x4;
typedef __amdgpu_buffer_rsrc_t rsrc_t;
#define LDSP __attribute__((address_space(3)))

constexpr int S_LEN = 2048, DM = 512;

constexpr unsigned WS_Q  = 0;
constexpr unsigned WS_UR = 2u << 20;
constexpr unsigned WS_O  = 4u << 20;
constexpr unsigned WS_YT = 6u << 20;
constexpr unsigned WS_KV = 8u << 20;
constexpr unsigned KF_BYTES = 65536, VF_BYTES = 64 * 1088, KV_BYTES = KF_BYTES + VF_BYTES;
constexpr unsigned WS_KN = WS_KV + KV_BYTES;
constexpr unsigned WS_G  = WS_KN + 1024;
constexpr unsigned WS_FY = WS_G + 8192;
constexpr unsigned WS_WO = 9u << 20;
constexpr unsigned WS_END = WS_WO + (512u << 10);
constexpr unsigned POISON = 0xffffffffu;

#define STAMP_DECL
#define STAMP(i)
#define STAMP_FLUSH(k)
constexpr float QSCALE = 0.25f * 1.4426950408889634f;

__device__ __forceinline__ rsrc_t mk_rsrc(const void* p, unsigned bytes) {
    return __builtin_amdgcn_make_buffer_rsrc((void*)p, (short)0, (int)bytes, 0x00020000);
}
__device__ __forceinline__ void dma16(rsrc_t rs, unsigned voff, unsigned soff, unsigned ldsaddr) {
    asm volatile("s_nop 4\n\ts_mov_b32 m0, %0\n\ts_nop 0\n\tbuffer_load_dwordx4 %1, %2, %3 offen lds"
                 :: "s"(ldsaddr), "v"(voff), "s"(rs), "s"(soff) : "memory");
}
__device__ __forceinline__ void bload16(u32x4& dst, rsrc_t rs, unsigned voff) {
    asm volatile("buffer_load_dwordx4 %0, %1, %2, 0 offen" : "=v"(dst) : "v"(voff), "s"(rs) : "memory");
}
__device__ __forceinline__ void bload16_sc1(u32x4& dst, rsrc_t rs, unsigned voff) {
    asm volatile("buffer_load_dwordx4 %0, %1, %2, 0 offen sc1" : "=v"(dst) : "v"(voff), "s"(rs) : "memory");
}
__device__ __forceinline__ void bload8(u32x2& dst, rsrc_t rs, unsigned voff) {
    asm volatile("buffer_load_dwordx2 %0, %1, %2, 0 offen" : "=v"(dst) : "v"(voff), "s"(rs) : "memory");
}
__device__ __forceinline__ void bload4(unsigned& dst, rsrc_t rs, unsigned voff) {
    asm volatile("buffer_load_dword %0, %1, %2, 0 offen" : "=v"(dst) : "v"(voff), "s"(rs) : "memory");
}
__device__ __forceinline__ void st16_sc1(rsrc_t rs, unsigned voff, u32x4 v) { __builtin_amdgcn_raw_buffer_store_b128(v, rs, voff, 0, 16); }
__device__ __forceinline__ void st8_sc1(rsrc_t rs, unsigned voff, u32x2 v) { __builtin_amdgcn_raw_buffer_store_b64(v, rs, voff, 0, 16); }
__device__ __forceinline__ u32x4 ld16_sc1(rsrc_t rs, unsigned voff) { return __builtin_amdgcn_raw_buffer_load_b128(rs, voff, 0, 16); }
template <int N> __device__ __forceinline__ void wait_vm() { asm volatile("s_waitcnt vmcnt(%0)" :: "i"(N) : "memory"); }
__device__ __forceinline__ void wait_lgkm0() { asm volatile("s_waitcnt lgkmcnt(0)" ::: "memory"); }
__device__ __forceinline__ void barrier() { asm volatile("" ::: "memory"); __builtin_amdgcn_s_barrier(); asm volatile("" ::: "memory"); }

__device__ __forceinline__ unsigned pack2(float a, float b) {
    f32x2 v = {a, b};
    return __builtin_bit_cast(unsigned, __builtin_convertvector(v, bf16x2));
}
__device__ __forceinline__ float u2f(unsigned u) { return __builtin_bit_cast(float, u); }
__device__ __forceinline__ float bflo(unsigned u) { return __builtin_bit_cast(float, u << 16); }
__device__ __forceinline__ float bfhi(unsigned u) { return __builtin_bit_cast(float, u & 0xffff0000u); }
__device__ __forceinline__ float rbf(float a) { return bflo(pack2(a, 0.f)); }

__device__ __forceinline__ u32x4 lds16(const char LDSP* p) { return *(const u32x4 LDSP*)p; }
__device__ __forceinline__ u32x2 ldstr(const char LDSP* p) {
    return __builtin_bit_cast(u32x2, __builtin_amdgcn_ds_read_tr16_b64_v4i16((s16x4 LDSP*)p));
}
__device__ __forceinline__ bf16x8 asbf(u32x4 v) { return __builtin_bit_cast(bf16x8, v); }
__device__ __forceinline__ u32x4 cat2(u32x2 a, u32x2 b) { return u32x4{a[0], a[1], b[0], b[1]}; }
__device__ __forceinline__ f32x16 mfma32(u32x4 a, u32x4 b, f32x16 c) { return __builtin_amdgcn_mfma_f32_32x32x16_bf16(asbf(a), asbf(b), c, 0, 0, 0); }
__device__ __forceinline__ f32x4 mfma16(u32x4 a, u32x4 b, f32x4 c) { return __builtin_amdgcn_mfma_f32_16x16x32_bf16(asbf(a), asbf(b), c, 0, 0, 0); }

constexpr unsigned G_XS = 0, G_WS = 65536;

struct GemmAddr { unsigned xa[4]; unsigned wa; };
__device__ __forceinline__ GemmAddr gemm_addr(int wm, int wn, int lane) {
    GemmAddr g;
    const unsigned row = 32 * wm + (lane & 31), h = lane >> 5;
#pragma unroll
    for (int s = 0; s < 4; ++s) g.xa[s] = G_XS + row * 128 + (((2 * s + h) ^ ((row >> 1) & 7)) << 4);
    const unsigned G = lane >> 4, q = (lane & 15) >> 2, p = lane & 3;
    const unsigned c = 4 * wn + 2 * (G & 1) + (p >> 1);
    g.wa = G_WS + (8 * (G >> 1) + q) * 128 + ((c ^ (((q >> 1) & 1) << 2)) << 4) + 8 * (p & 1);
    return g;
}
__device__ __forceinline__ void gemm_chunk(const char LDSP* sm, const GemmAddr& g, int kc, f32x16& acc) {
    u32x4 xb[4]; u32x2 w0[4], w1[4];
#pragma unroll
    for (int s = 0; s < 4; ++s) {
        xb[s] = lds16(sm + g.xa[s] + kc * 8192);
        w0[s] = ldstr(sm + g.wa + kc * 8192 + s * 2048);
        w1[s] = ldstr(sm + g.wa + kc * 8192 + s * 2048 + 512);
    }
#pragma unroll
    for (int s = 0; s < 4; ++s) acc = mfma32(cat2(w0[s], w1[s]), xb[s], acc);
}
__device__ __forceinline__ void gemm_stage(const char LDSP* sm, const GemmAddr& g, int kc0, f32x16& acc) {
    u32x4 xb[8]; u32x2 w0[8], w1[8];
#pragma unroll
    for (int s = 0; s < 8; ++s) {
        const int kc = kc0 + (s >> 2), ss = s & 3;
        xb[s] = lds16(sm + g.xa[ss] + kc * 8192);
        w0[s] = ldstr(sm + g.wa + kc * 8192 + ss * 2048);
        w1[s] = ldstr(sm + g.wa + kc * 8192 + ss * 2048 + 512);
    }
#pragma unroll
    for (int s = 0; s < 8; ++s) acc = mfma32(cat2(w0[s], w1[s]), xb[s], acc);
}
__device__ __forceinline__ void quads_to_rows(u32x2 (&p)[4], u32x4& r0, u32x4& r1) {
    auto s0 = __builtin_amdgcn_permlane32_swap(p[0][0], p[1][0], false, false);
    auto s1 = __builtin_amdgcn_permlane32_swap(p[0][1], p[1][1], false, false);
    auto s2 = __builtin_amdgcn_permlane32_swap(p[2][0], p[3][0], false, false);
    auto s3 = __builtin_amdgcn_permlane32_swap(p[2][1], p[3][1], false, false);
    r0 = u32x4{s0[0], s1[0], s0[1], s1[1]};
    r1 = u32x4{s2[0], s3[0], s2[1], s3[1]};
}
__device__ __forceinline__ unsigned wave_umax(unsigned v) {
    v = max(v, (unsigned)__builtin_amdgcn_update_dpp(0, (int)v, 0x111, 0xf, 0xf, true));
    v = max(v, (unsigned)__builtin_amdgcn_update_dpp(0, (int)v, 0x112, 0xf, 0xf, true));
    v = max(v, (unsigned)__builtin_amdgcn_update_dpp(0, (int)v, 0x114, 0xf, 0xf, true));
    v = max(v, (unsigned)__builtin_amdgcn_update_dpp(0, (int)v, 0x118, 0xf, 0xf, true));
    v = max(v, (unsigned)__builtin_amdgcn_update_dpp(0, (int)v, 0x142, 0xa, 0xf, false));
    v = max(v, (unsigned)__builtin_amdgcn_update_dpp(0, (int)v, 0x143, 0xc, 0xf, false));
    return (unsigned)__builtin_amdgcn_readlane((int)v, 63);
}

__device__ __forceinline__ unsigned lds_ld(const char LDSP* p) { return *(const volatile unsigned LDSP*)p; }
__device__ __forceinline__ void lds_wait(const char LDSP* p, unsigned n) {
    while ((unsigned)__builtin_amdgcn_readfirstlane((int)lds_ld(p)) < n) __builtin_amdgcn_s_sleep(1);
    asm volatile("" ::: "memory");
}
__device__ __forceinline__ void lds_post(char LDSP* p, int lane) {
    asm volatile("" ::: "memory");
    if (lane == 0) __hip_atomic_fetch_add((unsigned LDSP*)p, 1u, __ATOMIC_RELAXED, __HIP_MEMORY_SCOPE_WORKGROUP);
    asm volatile("" ::: "memory");
}
__device__ __forceinline__ unsigned lds_inc(char LDSP* p, int lane) {
    unsigned old = 0;
    asm volatile("" ::: "memory");
    if (lane == 0) old = __hip_atomic_fetch_add((unsigned LDSP*)p, 1u, __ATOMIC_RELAXED, __HIP_MEMORY_SCOPE_WORKGROUP);
    old = (unsigned)__builtin_amdgcn_readfirstlane((int)old);
    asm volatile("" ::: "memory");
    return old;
}
__device__ __forceinline__ void dma1(rsrc_t rs, unsigned voff, unsigned soff, unsigned ldsaddr) {
    asm volatile("s_mov_b32 m0, %0\n\ts_nop 0\n\tbuffer_load_dwordx4 %1, %2, %3 offen lds"
                 :: "s"(ldsaddr), "v"(voff), "s"(rs), "s"(soff) : "memory");
}
__device__ __forceinline__ void dma2(rsrc_t rs, unsigned voff, unsigned soff, unsigned ldsaddr) {
    asm volatile("s_mov_b32 m0, %0\n\ts_nop 0\n\tbuffer_load_dwordx4 %1, %2, %3 offen lds\n\tbuffer_load_dwordx4 %1, %2, %3 offen offset:1024 lds"
                 :: "s"(ldsaddr), "v"(voff), "s"(rs), "s"(soff) : "memory");
}
__device__ __forceinline__ void dma4(rsrc_t rs, unsigned voff, unsigned soff, unsigned ldsaddr) {
    asm volatile("s_mov_b32 m0, %0\n\ts_nop 0\n\tbuffer_load_dwordx4 %1, %2, %3 offen lds\n\tbuffer_load_dwordx4 %1, %2, %3 offen offset:1024 lds\n\t"
                 "buffer_load_dwordx4 %1, %2, %3 offen offset:2048 lds\n\tbuffer_load_dwordx4 %1, %2, %3 offen offset:3072 lds"
                 :: "s"(ldsaddr), "v"(voff), "s"(rs), "s"(soff) : "memory");
}
__device__ __forceinline__ float dot2(unsigned a, unsigned b, float c) {
    return __builtin_amdgcn_fdot2_f32_bf16(__builtin_bit_cast(bf16x2, a), __builtin_bit_cast(bf16x2, b), c, false);
}

constexpr unsigned K1_WE = 131072;
constexpr unsigned K1_KT = K1_WE + 16384;
constexpr unsigned K1_WO = K1_KT + 8192;
constexpr unsigned K1_CNT = K1_WO + 2048;
constexpr unsigned K1_LDS = K1_CNT + 64;
enum { D_KT = 0, D_S0 = 1, D_S1 = 2, D_S2 = 3, D_S3 = 4, D_KRD = 5, D_TS = 6, D_ALL = 7 };

}
extern "C" __global__ void gm_k2_main(const unsigned short* __restrict__ x, const unsigned short* __restrict__ ssmk, const unsigned short* __restrict__ ssmD,
                                      char* __restrict__ ws, unsigned short* __restrict__ out);
extern "C" __global__ __launch_bounds__(512) void gm_k1_proj(const u16* __restrict__ x, const u16* __restrict__ kin, const u16* __restrict__ vin,
                                               const u16* __restrict__ shiftk, const u16* __restrict__ wq, const u16* __restrict__ wk,
                                               const u16* __restrict__ wv, const u16* __restrict__ wo, const u16* __restrict__ gw,
                                               const u16* __restrict__ ssmk, const u16* __restrict__ outp, char* __restrict__ ws) {
    __shared__ __attribute__((aligned(16))) char smem[K1_LDS];
    const char LDSP* sm = (const char LDSP*)smem;
    char LDSP* smw = (char LDSP*)smem;
    const unsigned lds0 = (unsigned)(size_t)sm;
    const int lane = threadIdx.x & 63;
    const int w = __builtin_amdgcn_readfirstlane(threadIdx.x >> 6);
    const int bid = blockIdx.x;
    const int tt = (bid & 7) * 4 + (bid >> 6), j = (bid >> 3) & 7;
    asm volatile("" :: "s"(x), "s"(kin), "s"(vin), "s"(shiftk), "s"(wq), "s"(wk), "s"(wv), "s"(wo), "s"(gw), "s"(ssmk), "s"(outp), "s"(ws));
    const rsrc_t rsWS = mk_rsrc(ws, WS_END);
    STAMP_DECL;
    STAMP(0);
    if (w == 0) { if (lane < 16) *(unsigned LDSP*)(smw + K1_CNT + lane * 4) = 0; wait_lgkm0(); }
    barrier();

    if (w >= 4) {
        const int lw = w - 4;
        const rsrc_t rsX = mk_rsrc(x, S_LEN * DM * 2), rsW = mk_rsrc(wq, DM * DM * 2);
        const unsigned gr = lane >> 3, gc = lane & 7;
        const unsigned row0 = 16 * lw + gr;
        const unsigned vx0 = (tt * 64 + row0) * 1024 + ((gc ^ ((row0 >> 1) & 7)) << 4);
        const unsigned vx1 = (tt * 64 + row0 + 8) * 1024 + ((gc ^ (((row0 + 8) >> 1) & 7)) << 4);
        const unsigned vw0 = row0 * 1024 + j * 128 + ((gc ^ (((gr >> 1) & 1) << 2)) << 4);
        auto chunk = [&](int kc) {
            dma1(rsX, vx0, kc * 128, lds0 + G_XS + kc * 8192 + lw * 2048);
            dma1(rsX, vx1, kc * 128, lds0 + G_XS + kc * 8192 + lw * 2048 + 1024);
            dma1(rsW, vw0, kc * 65536, lds0 + G_WS + kc * 8192 + lw * 2048);
            dma1(rsW, vw0, kc * 65536 + 8192, lds0 + G_WS + kc * 8192 + lw * 2048 + 1024);
        };
        if (j < 2) {
            const rsrc_t rsE = mk_rsrc(j == 0 ? wk : wv, DM * 16 * 2);
#pragma unroll
            for (int i = 0; i < 4; ++i) {
                const unsigned prow = (lw * 4 + i) * 32 + (lane >> 1);
                const unsigned grow = prow ^ (((prow >> 3) & 1) << 2);
                dma1(rsE, grow * 32 + (lane & 1) * 16, 0, lds0 + K1_WE + (lw * 4 + i) * 1024);
            }
        } else if (j == 2) {
            if (lw == 0) dma1(mk_rsrc(gw, DM * 2), lane * 16, 0, lds0 + K1_WE);
        }
        if (lw >= 2)
            dma1(mk_rsrc(wo, DM * DM * 2), (32 * (bid & 15) + 16 * (lw - 2) + (lane >> 2)) * 1024 + (bid >> 4) * 64 + (lane & 3) * 16, 0,
                 lds0 + K1_WO + (lw - 2) * 1024);
        STAMP(4);
        chunk(0); chunk(1); chunk(2); chunk(3);
        STAMP(5);
        chunk(4); chunk(5);
        wait_vm<16>(); lds_post(smw + K1_CNT + 4 * D_S0, lane);
        STAMP(6);
        chunk(6); chunk(7);
        wait_vm<16>(); lds_post(smw + K1_CNT + 4 * D_S1, lane);
        wait_vm<8>();  lds_post(smw + K1_CNT + 4 * D_S2, lane);
        wait_vm<4>();  lds_post(smw + K1_CNT + 4 * D_S3, lane);
        wait_vm<0>();  lds_post(smw + K1_CNT + 4 * D_ALL, lane);
        STAMP(1);
        st16_sc1(rsWS, WS_O + bid * 8192 + lw * 2048 + lane * 16, u32x4{POISON, POISON, POISON, POISON});
        st16_sc1(rsWS, WS_O + bid * 8192 + lw * 2048 + 1024 + lane * 16, u32x4{POISON, POISON, POISON, POISON});
        if (lw == 0 && lane == 0) __builtin_amdgcn_raw_buffer_store_b32(0u, rsWS, WS_FY + bid * 4, 0, 16);
        lds_wait(sm + K1_CNT + 4 * D_TS, 4);
#pragma unroll
        for (int i = 0; i < 2; ++i) {
            const unsigned ch = 16 * lw + 8 * i + gr;
            st16_sc1(rsWS, WS_UR + (j * 64 + ch) * 4096 + (1984 - 64 * tt + 8 * gc) * 2, lds16(sm + K1_KT + ch * 128 + ((gc ^ (ch >> 3)) << 4)));
        }
        lds_wait(sm + K1_CNT + 4 * D_ALL, 4);
        STAMP(2);
        if (lw >= 2) {
            const unsigned G = lane >> 4, q = (lane & 15) >> 2, p = lane & 3;
            const unsigned a = K1_WO + (16 * (lw - 2) + 8 * (G >> 1) + q) * 64 + (16 * (G & 1) + 4 * p) * 2;
            st16_sc1(rsWS, WS_WO + ((bid >> 4) * 32 + 2 * (bid & 15) + (lw - 2)) * 1024 + lane * 16, cat2(ldstr(sm + a), ldstr(sm + a + 256)));
        }
        if (j < 3) {
            const unsigned row = 16 * lw + (lane & 15), g = lane >> 4;
            const unsigned ex0 = G_XS + row * 128 + ((g ^ ((row >> 1) & 7)) << 4), ex1 = G_XS + row * 128 + (((4 + g) ^ ((row >> 1) & 7)) << 4);
            const unsigned q = (lane & 15) >> 2, p = lane & 3, n = lane & 15;
            const unsigned ee0 = K1_WE + (8 * g + q + 4 * (0 ^ (g & 1))) * 32 + 8 * p, ee1 = K1_WE + (8 * g + q + 4 * (1 ^ (g & 1))) * 32 + 8 * p;
            f32x4 e0 = {}, e1 = {};
            if (j == 0) {
#pragma unroll
                for (int kc = 0; kc < 8; ++kc) {
                    e0 = mfma16(cat2(ldstr(sm + ee0 + kc * 2048), ldstr(sm + ee1 + kc * 2048)), lds16(sm + ex0 + kc * 8192), e0);
                    e1 = mfma16(cat2(ldstr(sm + ee0 + kc * 2048 + 1024), ldstr(sm + ee1 + kc * 2048 + 1024)), lds16(sm + ex1 + kc * 8192), e1);
                }
                const unsigned p0 = pack2(e0[0] + e1[0], e0[1] + e1[1]), p1 = pack2(e0[2] + e1[2], e0[3] + e1[3]);
                const int key = tt * 64 + 16 * lw + n;
                *(u32x2*)(ws + WS_KV + (key >> 5) * 1024 + (g >> 1) * 512 + (key & 31) * 16 + (g & 1) * 8) = u32x2{p0, p1};
                float ss = dot2(p0, p0, dot2(p1, p1, 0.f));
                ss += __shfl_xor(ss, 16); ss += __shfl_xor(ss, 32);
                const unsigned m = wave_umax(__builtin_bit_cast(unsigned, ss));
                if (lane == 0) ((unsigned*)(ws + WS_KN))[tt * 4 + lw] = m;
            } else if (j == 1) {
#pragma unroll
                for (int kc = 0; kc < 8; ++kc) {
                    e0 = mfma16(lds16(sm + ex0 + kc * 8192), cat2(ldstr(sm + ee0 + kc * 2048), ldstr(sm + ee1 + kc * 2048)), e0);
                    e1 = mfma16(lds16(sm + ex1 + kc * 8192), cat2(ldstr(sm + ee0 + kc * 2048 + 1024), ldstr(sm + ee1 + kc * 2048 + 1024)), e1);
                }
                const unsigned p0 = pack2(e0[0] + e1[0], e0[1] + e1[1]), p1 = pack2(e0[2] + e1[2], e0[3] + e1[3]);
                const int tile = 2 * tt + (lw >> 1), pp = lw & 1;
                char* vb = ws + WS_KV + KF_BYTES + tile * 1088 + pp * 544;
                *(u32x2*)(vb + ((g >> 1) * 16 + n) * 16 + (g & 1) * 8) = u32x2{p0, p1};
                if (lane < 2) {
                    const unsigned f = lane == 0 ? 0x3f803f80u : 0u;
                    *(u32x4*)(vb + (32 + lane) * 16) = u32x4{f, f, f, f};
                }
                const unsigned a0 = p0 & 0x7fff7fffu, a1 = p1 & 0x7fff7fffu;
                const unsigned m = wave_umax(max(max(a0 & 0xffffu, a0 >> 16), max(a1 & 0xffffu, a1 >> 16)));
                if (lane == 0) ((unsigned*)(ws + WS_KN))[128 + tt * 4 + lw] = m << 16;
            } else {
#pragma unroll
                for (int kc = 0; kc < 8; ++kc) {
                    e0 = mfma16(lds16(sm + ex0 + kc * 8192), lds16(sm + K1_WE + g * 16 + kc * 128), e0);
                    e1 = mfma16(lds16(sm + ex1 + kc * 8192), lds16(sm + K1_WE + g * 16 + kc * 128 + 64), e1);
                }
                if (n == 0) {
                    f32x4 gv;
#pragma unroll
                    for (int r = 0; r < 4; ++r) gv[r] = rbf(1.f / (1.f + __expf(-rbf(e0[r] + e1[r]))));
                    *(f32x4*)(ws + WS_G + (tt * 64 + 16 * lw + 4 * g) * 4) = gv;
                }
            }
        }
        STAMP(3);
    } else {
        unsigned warm0 = 0, warm1 = 0;
        const rsrc_t rsV = mk_rsrc(vin, S_LEN * DM * 2), rsS = mk_rsrc(shiftk, DM * 4 * 2);
        const int oct = lane & 7, t0 = 16 * w + 2 * (lane >> 3);
        u32x4 va, vb, sk0, sk1, sk2, sk3;
        u32x4 R0, R1, R2, R3, R4;
        {
            const rsrc_t rsK = mk_rsrc(kin, S_LEN * DM * 2);
            const unsigned vk = (unsigned)((tt * 64 - 3 + t0) * 1024 + j * 128 + oct * 16);
            bload16(R0, rsK, vk); bload16(R1, rsK, vk + 1024); bload16(R2, rsK, vk + 2048); bload16(R3, rsK, vk + 3072); bload16(R4, rsK, vk + 4096);
        }
        bload16(va, rsV, (tt * 64 + t0) * 1024 + j * 128 + oct * 16);
        bload16(vb, rsV, (tt * 64 + t0 + 1) * 1024 + j * 128 + oct * 16);
        {
            const unsigned so = (j * 64 + oct * 8) * 8;
            bload16(sk0, rsS, so); bload16(sk1, rsS, so + 16); bload16(sk2, rsS, so + 32); bload16(sk3, rsS, so + 48);
        }
        const int wm = w & 1, wn = w >> 1;
        const GemmAddr ga = gemm_addr(wm, wn, lane);
        STAMP(6);
        asm volatile("s_waitcnt vmcnt(0)" : "+v"(va), "+v"(vb), "+v"(sk0), "+v"(sk1), "+v"(sk2), "+v"(sk3), "+v"(R0), "+v"(R1), "+v"(R2), "+v"(R3), "+v"(R4), "+v"(warm0), "+v"(warm1) :: "memory");
        STAMP(1);
        {
            const u32x4 R[5] = {R0, R1, R2, R3, R4};
            const u32x4 sk[4] = {sk0, sk1, sk2, sk3};
            unsigned tw[8];
#pragma unroll
            for (int c = 0; c < 8; ++c) {
                const int d = c >> 1;
                const unsigned sel = (c & 1) ? 0x07060302u : 0x05040100u;
                const unsigned s01 = sk[d][(c & 1) * 2], s23 = sk[d][(c & 1) * 2 + 1];
                const float a0 = dot2(__builtin_amdgcn_perm(R[2][d], R[3][d], sel), s01, dot2(__builtin_amdgcn_perm(R[0][d], R[1][d], sel), s23, 0.f));
                const float a1 = dot2(__builtin_amdgcn_perm(R[3][d], R[4][d], sel), s01, dot2(__builtin_amdgcn_perm(R[1][d], R[2][d], sel), s23, 0.f));
                const float v0 = (c & 1) ? bfhi(va[d]) : bflo(va[d]), v1 = (c & 1) ? bfhi(vb[d]) : bflo(vb[d]);
                tw[c] = pack2(a1 * v1, a0 * v0);
            }
            const unsigned mp = 62 - t0;
#pragma unroll
            for (int c = 0; c < 8; ++c)
                *(unsigned LDSP*)(smw + K1_KT + (8 * oct + c) * 128 + (((mp >> 3) ^ oct) << 4) + (mp & 7) * 2) = tw[c];
            wait_lgkm0();
            lds_post(smw + K1_CNT + 4 * D_TS, lane);
        }
        STAMP(2);
        f32x16 acc = {};
        lds_wait(sm + K1_CNT + 4 * D_S0, 4); gemm_stage(sm, ga, 0, acc);
        lds_wait(sm + K1_CNT + 4 * D_S1, 4); gemm_stage(sm, ga, 2, acc);
        STAMP(3);
        lds_wait(sm + K1_CNT + 4 * D_S2, 4); gemm_stage(sm, ga, 4, acc);
        lds_wait(sm + K1_CNT + 4 * D_S3, 4); gemm_chunk(sm, ga, 6, acc);
        lds_wait(sm + K1_CNT + 4 * D_ALL, 4); gemm_chunk(sm, ga, 7, acc);
        STAMP(4);
        u32x2 p[4];
#pragma unroll
        for (int q = 0; q < 4; ++q) {
            const unsigned a = pack2(acc[4 * q], acc[4 * q + 1]), b = pack2(acc[4 * q + 2], acc[4 * q + 3]);
            p[q][0] = pack2(bflo(a) * QSCALE, bfhi(a) * QSCALE);
            p[q][1] = pack2(bflo(b) * QSCALE, bfhi(b) * QSCALE);
        }
        u32x4 r0, r1;
        quads_to_rows(p, r0, r1);
        const unsigned qo = WS_Q + (tt * 64 + 32 * wm + (lane & 31)) * 1024 + (j * 64 + 32 * wn + 8 * (lane >> 5)) * 2;
        st16_sc1(rsWS, qo, r0);
        st16_sc1(rsWS, qo + 32, r1);
        STAMP(5);
    }
    STAMP_FLUSH(0);
}

constexpr unsigned K2_K = 0, K2_V = 65536, K2_H = KV_BYTES, K2_HC = 4160, K2_U = K2_H + 4 * K2_HC, K2_UC = 4608;
constexpr unsigned K2_CNT = K2_U + 2 * K2_UC;
constexpr unsigned K2_LDS = K2_CNT + 64;
constexpr unsigned K2_XP = K2_H, K2_YP = K2_H + 8192;
constexpr unsigned K2_PART = 0;
constexpr unsigned K2_OI = 32768;
#define XSLEEP 10
enum { C_ST0 = 0, C_ST1 = 1, C_ST2 = 2, C_ST3 = 3, C_CH0 = 4, C_CH1 = 5, C_YST = 6, C_SLOW = 8   };

#define AE(X, i) "v_exp_f32 v[" #X "+" #i "], v[" #X "+" #i "]\n\t"
#define ASTEP(X0, X1, Y0, Y1, t)                                                             \
    AE(X0, 0) AE(X0, 1) AE(X0, 2) AE(X0, 3)                                                  \
    "s_waitcnt lgkmcnt(2)\n\t"                                                               \
    "v_mfma_f32_32x32x16_bf16 v[" #Y0 ":" #Y0 "+15], %2, %10, 0\n\t"                         \
    AE(X0, 4) AE(X0, 5) AE(X0, 6) AE(X0, 7)                                                  \
    "v_mfma_f32_32x32x16_bf16 v[" #Y1 ":" #Y1 "+15], %2, %11, 0\n\t"                         \
    "ds_read_b128 %2, %5 offset:(" #t "+2)*1024\n\t"                                         \
    AE(X0, 8) AE(X0, 9) AE(X0, 10) AE(X0, 11)                                                \
    "s_waitcnt lgkmcnt(2)\n\t"                                                               \
    "v_smfmac_f32_32x32x32_bf16 %0, %3, v[" #X0 ":" #X0 "+7], %12\n\t"                       \
    AE(X0, 12) AE(X0, 13) AE(X0, 14) AE(X0, 15) AE(X1, 0) AE(X1, 1)                          \
    "s_waitcnt lgkmcnt(1)\n\t"                                                               \
    "v_smfmac_f32_32x32x32_bf16 %0, %4, v[" #X0 "+8:" #X0 "+15], %12\n\t"                    \
    AE(X1, 2) AE(X1, 3) AE(X1, 4) AE(X1, 5) AE(X1, 6) AE(X1, 7) AE(X1, 8) AE(X1, 9)          \
    "v_smfmac_f32_32x32x32_bf16 %1, %3, v[" #X1 ":" #X1 "+7], %12\n\t"                       \
    "ds_read_b128 %3, %6 offset:(" #t "+1)*1088\n\t"                                         \
    AE(X1, 10) AE(X1, 11) AE(X1, 12) AE(X1, 13) AE(X1, 14) AE(X1, 15)                        \
    "s_nop 1\n\t"                                                                            \
    "v_smfmac_f32_32x32x32_bf16 %1, %4, v[" #X1 "+8:" #X1 "+15], %12\n\t"                    \
    "ds_read_b128 %4, %6 offset:(" #t "+1)*1088+544\n\t"
#define ASTEP_E(t) ASTEP(0, 16, 32, 48, t)
#define ASTEP_O(t) ASTEP(32, 48, 0, 16, t)
#define AWAIT(lbl, off)                                                                      \
    #lbl ":\n\t"                                                                             \
    "ds_read_b32 %7, %13 offset:" #off "\n\t"                                                \
    "s_waitcnt lgkmcnt(0)\n\t"                                                               \
    "v_readfirstlane_b32 %8, %7\n\t"                                                         \
    "s_nop 3\n\t"                                                                            \
    "s_cmp_ge_u32 %8, 4\n\t"                                                                 \
    "s_cbranch_scc1 " #lbl "9f\n\t"                                                          \
    "s_sleep 1\n\t"                                                                          \
    "s_branch " #lbl "b\n\t"                                                                 \
    #lbl "9:\n\t"

__device__ __forceinline__ void attn_fast(f32x16& O0, f32x16& O1, u32x4 q0, u32x4 q1, unsigned kaddr, unsigned vaddr, unsigned caddr) {
    u32x4 kf, v0, v1;
    unsigned idx = 0xddddu, tv; int ts, it = 0;
    asm volatile(
        "s_setprio 2\n\t"
        AWAIT(1, 0)
        "ds_read_b128 %2, %5\n\t"
        "s_waitcnt lgkmcnt(0)\n\t"
        "v_mfma_f32_32x32x16_bf16 v[0:15], %2, %10, 0\n\t"
        "v_mfma_f32_32x32x16_bf16 v[16:31], %2, %11, 0\n\t"
        "ds_read_b128 %2, %5 offset:1024\n\t"
        "ds_read_b128 %3, %6\n\t"
        "ds_read_b128 %4, %6 offset:544\n\t"
        "s_nop 10\n\t"
        "4:\n\t"
        ASTEP_E(0) ASTEP_O(1)
        "s_cmp_gt_u32 %9, 1\n\t"
        "s_cbranch_scc1 5f\n\t"
        "s_cmp_eq_u32 %9, 0\n\t"
        "s_cbranch_scc0 7f\n\t"
        AWAIT(2, 4)
        "s_branch 5f\n\t"
        "7:\n\t"
        AWAIT(8, 12)
        "5:\n\t"
        ASTEP_E(2) ASTEP_O(3) ASTEP_E(4) ASTEP_O(5)
        "s_cmp_lg_u32 %9, 0\n\t"
        "s_cbranch_scc1 6f\n\t"
        AWAIT(3, 8)
        "6:\n\t"
        ASTEP_E(6) ASTEP_O(7)
        "v_add_u32 %5, 0x2000, %5\n\t"
        "v_add_u32 %6, 0x2200, %6\n\t"
        "s_add_u32 %9, %9, 1\n\t"
        "s_cmp_eq_u32 %9, 2\n\t"
        "s_cbranch_scc0 10f\n\t"
        "s_setprio 1\n\t"
        "10:\n\t"
        "s_cmp_eq_u32 %9, 3\n\t"
        "s_cbranch_scc0 11f\n\t"
        "s_setprio 0\n\t"
        "11:\n\t"
        "s_cmp_lg_u32 %9, 4\n\t"
        "s_cbranch_scc1 4b\n\t"
        "s_waitcnt lgkmcnt(0)\n\t"
        "s_nop 15\n\t"
        "s_nop 7\n\t"
        : "+v"(O0), "+v"(O1), "=&v"(kf), "=&v"(v0), "=&v"(v1), "+v"(kaddr), "+v"(vaddr), "=&v"(tv), "=&s"(ts), "+s"(it)
        : "v"(q0), "v"(q1), "v"(idx), "v"(caddr)
        : "v0", "v1", "v2", "v3", "v4", "v5", "v6", "v7", "v8", "v9", "v10", "v11", "v12", "v13", "v14", "v15",
          "v16", "v17", "v18", "v19", "v20", "v21", "v22", "v23", "v24", "v25", "v26", "v27", "v28", "v29", "v30", "v31",
          "v32", "v33", "v34", "v35", "v36", "v37", "v38", "v39", "v40", "v41", "v42", "v43", "v44", "v45", "v46", "v47",
          "v48", "v49", "v50", "v51", "v52", "v53", "v54", "v55", "v56", "v57", "v58", "v59", "v60", "v61", "v62", "v63",
          "scc", "memory");
}

__device__ __forceinline__ void attn_slow(f32x16& O, float& m, u32x4 q, const char LDSP* sm, unsigned kaddr, unsigned vaddr) {
#pragma unroll 1
    for (int t = 0; t < 32; ++t) {
        const u32x4 kf = lds16(sm + kaddr + t * 1024);
        const u32x4 v0 = lds16(sm + vaddr + t * 1088), v1 = lds16(sm + vaddr + t * 1088 + 544);
        f32x16 s = mfma32(kf, q, f32x16{});
        float tm = s[0];
#pragma unroll
        for (int r = 1; r < 16; ++r) tm = fmaxf(tm, s[r]);
        tm = fmaxf(tm, __shfl_xor(tm, 32));
        const float mn = fmaxf(m, tm);
        const float alpha = __builtin_amdgcn_exp2f(m - mn);
        m = mn;
#pragma unroll
        for (int r = 0; r < 16; ++r) { O[r] *= alpha; s[r] = __builtin_amdgcn_exp2f(s[r] - mn); }
        const f32x8 slo = __builtin_shufflevector(s, s, 0, 1, 2, 3, 4, 5, 6, 7), shi = __builtin_shufflevector(s, s, 8, 9, 10, 11, 12, 13, 14, 15);
        O = __builtin_amdgcn_smfmac_f32_32x32x32_bf16(asbf(v0), __builtin_bit_cast(bf16x16, slo), O, 0xdddd, 0, 0);
        O = __builtin_amdgcn_smfmac_f32_32x32x32_bf16(asbf(v1), __builtin_bit_cast(bf16x16, shi), O, 0xdddd, 0, 0);
    }
}

template <int SET>
__device__ __forceinline__ void conv_wave(const char LDSP* sm, int ch, int lane, float dskip, rsrc_t rsWS, unsigned yoff) {
    constexpr int T0 = SET ? 1 : 0, T1 = SET ? 2 : 3, T2 = SET ? 5 : 4, T3 = SET ? 6 : 7;
    constexpr int NP = T3 + 1;
    const int b = lane & 15, g = lane >> 4;
    const int c = (b + 1) & 1;
    unsigned ha[4];
#pragma unroll
    for (int i = 0; i < 4; ++i) {
        ha[i] = K2_H + (ch * 2 + c) * K2_HC + ((b + 1 - c) / 2 + 4 * g) * 4 + i * 1024;
        asm volatile("" : "+v"(ha[i]));
    }
    unsigned ua = K2_U + ch * K2_UC + (2032 - 16 * b + 8 * g) * 2 - 7 * 512;
    asm volatile("" : "+v"(ua));
    f32x4 acc[4] = {};
#pragma unroll
    for (int r = 0; r < 8; ++r) {
        u32x4 A[NP], B[NP];
#pragma unroll
        for (int p = 0; p < NP; ++p) {
            const unsigned LDSP* hp = (const unsigned LDSP*)(sm + ha[p >> 1] + r * 64 + (p & 1) * 512);
            A[p] = u32x4{hp[0], hp[1], hp[2], hp[3]};
            B[p] = lds16(sm + ua + r * 64 + (7 - p) * 512);
        }
#pragma unroll
        for (int p = 0; p < NP; ++p) {
            if (p <= T0) acc[0] = mfma16(A[p], B[T0 - p < 0 ? 0 : T0 - p], acc[0]);
            if (p <= T1) acc[1] = mfma16(A[p], B[T1 - p < 0 ? 0 : T1 - p], acc[1]);
            if (p <= T2) acc[2] = mfma16(A[p], B[T2 - p < 0 ? 0 : T2 - p], acc[2]);
            if (p <= T3) acc[3] = mfma16(A[p], B[T3 - p], acc[3]);
        }
    }
    constexpr int TI[4] = {T0, T1, T2, T3};
#pragma unroll
    for (int i = 0; i < 4; ++i) {
        const int s = 256 * TI[i] + 16 * b + 4 * g;
        const u32x2 u = *(const u32x2 LDSP*)(sm + K2_U + ch * K2_UC + (2044 - s) * 2);
        const float y0 = rbf(acc[i][0]) + rbf(bfhi(u[1]) * dskip), y1 = rbf(acc[i][1]) + rbf(bflo(u[1]) * dskip);
        const float y2 = rbf(acc[i][2]) + rbf(bfhi(u[0]) * dskip), y3 = rbf(acc[i][3]) + rbf(bflo(u[0]) * dskip);
        st8_sc1(rsWS, yoff + s * 2, u32x2{pack2(y0, y1), pack2(y2, y3)});
    }
}

template <int I0>
__device__ __forceinline__ void oproj12(f32x16& acc, const u32x4* wf, unsigned ob) {
    u32x4 t0, t1, t2, t3; unsigned a;
    asm volatile(
        "v_xor_b32 %5, (((%c19+0)&7)<<5), %18\n\t" "ds_read_b128 %1, %5 offset:((%c19+0)>>3)*256\n\t"
        "v_xor_b32 %5, (((%c19+1)&7)<<5), %18\n\t" "ds_read_b128 %2, %5 offset:((%c19+1)>>3)*256\n\t"
        "v_xor_b32 %5, (((%c19+2)&7)<<5), %18\n\t" "ds_read_b128 %3, %5 offset:((%c19+2)>>3)*256\n\t"
        "v_xor_b32 %5, (((%c19+3)&7)<<5), %18\n\t" "ds_read_b128 %4, %5 offset:((%c19+3)>>3)*256\n\t"
        "s_waitcnt lgkmcnt(3)\n\t" "v_mfma_f32_32x32x16_bf16 %0, %6, %1, %0\n\t"
        "v_xor_b32 %5, (((%c19+4)&7)<<5), %18\n\t" "ds_read_b128 %1, %5 offset:((%c19+4)>>3)*256\n\t"
        "s_waitcnt lgkmcnt(3)\n\t" "v_mfma_f32_32x32x16_bf16 %0, %7, %2, %0\n\t"
        "v_xor_b32 %5, (((%c19+5)&7)<<5), %18\n\t" "ds_read_b128 %2, %5 offset:((%c19+5)>>3)*256\n\t"
        "s_waitcnt lgkmcnt(3)\n\t" "v_mfma_f32_32x32x16_bf16 %0, %8, %3, %0\n\t"
        "v_xor_b32 %5, (((%c19+6)&7)<<5), %18\n\t" "ds_read_b128 %3, %5 offset:((%c19+6)>>3)*256\n\t"
        "s_waitcnt lgkmcnt(3)\n\t" "v_mfma_f32_32x32x16_bf16 %0, %9, %4, %0\n\t"
        "v_xor_b32 %5, (((%c19+7)&7)<<5), %18\n\t" "ds_read_b128 %4, %5 offset:((%c19+7)>>3)*256\n\t"
        "s_waitcnt lgkmcnt(3)\n\t" "v_mfma_f32_32x32x16_bf16 %0, %10, %1, %0\n\t"
        "v_xor_b32 %5, (((%c19+8)&7)<<5), %18\n\t" "ds_read_b128 %1, %5 offset:((%c19+8)>>3)*256\n\t"
        "s_waitcnt lgkmcnt(3)\n\t" "v_mfma_f32_32x32x16_bf16 %0, %11, %2, %0\n\t"
        "v_xor_b32 %5, (((%c19+9)&7)<<5), %18\n\t" "ds_read_b128 %2, %5 offset:((%c19+9)>>3)*256\n\t"
        "s_waitcnt lgkmcnt(3)\n\t" "v_mfma_f32_32x32x16_bf16 %0, %12, %3, %0\n\t"
        "v_xor_b32 %5, (((%c19+10)&7)<<5), %18\n\t" "ds_read_b128 %3, %5 offset:((%c19+10)>>3)*256\n\t"
        "s_waitcnt lgkmcnt(3)\n\t" "v_mfma_f32_32x32x16_bf16 %0, %13, %4, %0\n\t"
        "v_xor_b32 %5, (((%c19+11)&7)<<5), %18\n\t" "ds_read_b128 %4, %5 offset:((%c19+11)>>3)*256\n\t"
        "s_waitcnt lgkmcnt(3)\n\t" "v_mfma_f32_32x32x16_bf16 %0, %14, %1, %0\n\t"
        "s_waitcnt lgkmcnt(2)\n\t" "v_mfma_f32_32x32x16_bf16 %0, %15, %2, %0\n\t"
        "s_waitcnt lgkmcnt(1)\n\t" "v_mfma_f32_32x32x16_bf16 %0, %16, %3, %0\n\t"
        "s_waitcnt lgkmcnt(0)\n\t" "v_mfma_f32_32x32x16_bf16 %0, %17, %4, %0\n\t"
        "s_nop 15\n\t"
        : "+v"(acc), "=&v"(t0), "=&v"(t1), "=&v"(t2), "=&v"(t3), "=&v"(a)
        : "v"(wf[0]), "v"(wf[1]), "v"(wf[2]), "v"(wf[3]), "v"(wf[4]), "v"(wf[5]), "v"(wf[6]), "v"(wf[7]), "v"(wf[8]), "v"(wf[9]), "v"(wf[10]), "v"(wf[11]),
          "v"(ob), "i"(I0)
        : "memory");
}

extern "C" __global__ __launch_bounds__(768) void gm_k2_main(const u16* __restrict__ x, const u16* __restrict__ ssmk, const u16* __restrict__ ssmD,
                                               char* __restrict__ ws, u16* __restrict__ out) {
    __shared__ __attribute__((aligned(16))) char smem[K2_LDS];
    const char LDSP* sm = (const char LDSP*)smem;
    char LDSP* smw = (char LDSP*)smem;
    const unsigned lds0 = (unsigned)(size_t)sm;
    const int lane = threadIdx.x & 63;
    const int w = __builtin_amdgcn_readfirstlane(threadIdx.x >> 6);
    const int bid = blockIdx.x;
    const int tb = (bid & 7) * 8 + (bid >> 5), hg = (bid >> 3) & 3;
    asm volatile("" :: "s"(x), "s"(ssmk), "s"(ssmD), "s"(ws), "s"(out));
    const rsrc_t rsWS = mk_rsrc(ws, WS_END);
    const int r = lane & 31, h = lane >> 5;
    STAMP_DECL;
    STAMP(0);

    if (w < 8) {
        const int rg = w & 3, kh = w >> 2;
        u32x4 q0, q1;
        const unsigned qoff = WS_Q + (unsigned)(tb * 32 + 8 * rg + (r >> 3)) * 1024 + (8 * hg + (r & 7)) * 32 + h * 16;
        bload16(q0, rsWS, qoff + kh * 4096);
        bload16(q1, rsWS, qoff + (kh ^ 1) * 4096);
        const unsigned kaddr = K2_K + kh * 32 * 1024 + lane * 16;
        const unsigned vaddr = K2_V + kh * 32 * 1088 + (r < 16 ? (h * 16 + r) * 16 : ((r & 11) == 0 ? 32 * 16 : 33 * 16));
        barrier();
        asm volatile("s_waitcnt vmcnt(0)" : "+v"(q0), "+v"(q1) :: "memory");
        f32x16 O0 = {}, O1 = {};
        STAMP(1);
        attn_fast(O0, O1, q0, q1, lds0 + kaddr, lds0 + vaddr, lds0 + K2_CNT);
        STAMP(2);
        barrier();
        float m0 = 0.f, m1 = 0.f;
        const u32x4 slow4 = lds16(sm + K2_CNT + 4 * C_SLOW);
        const bool anyslow = __builtin_amdgcn_readfirstlane((int)(slow4[0] | slow4[1] | slow4[2] | slow4[3])) != 0;
        bool slow = false;
        if (anyslow) {
            slow = __builtin_amdgcn_readfirstlane((int)lds_ld(sm + K2_CNT + 4 * (C_SLOW + rg))) != 0;
            if (slow) {
                O0 = f32x16{}; O1 = f32x16{};
                m0 = m1 = -__builtin_inff();
                attn_slow(O0, m0, q0, sm, kaddr, vaddr);
                attn_slow(O1, m1, q1, sm, kaddr, vaddr);
            }
            barrier();
        }
        STAMP(3);
        {
            char LDSP* pw = smw + K2_PART + w * 2560;
            *(f32x4 LDSP*)(pw + lane * 16) = f32x4{O1[0], O1[1], O1[2], O1[3]};
            *(f32x4 LDSP*)(pw + 1024 + lane * 16) = f32x4{O1[4], O1[5], O1[6], O1[7]};
            *(f32x2 LDSP*)(pw + 2048 + lane * 8) = f32x2{O1[8], m1};
        }
        wait_lgkm0();
        barrier();
        STAMP(4);
        u32x4 o;
        {
            const char LDSP* pr = sm + K2_PART + (w ^ 4) * 2560;
            f32x4 pa = *(const f32x4 LDSP*)(pr + lane * 16), pb = *(const f32x4 LDSP*)(pr + 1024 + lane * 16);
            const f32x2 lm = *(const f32x2 LDSP*)(pr + 2048 + lane * 8);
            f32x4 ka = f32x4{O0[0], O0[1], O0[2], O0[3]}, kb = f32x4{O0[4], O0[5], O0[6], O0[7]};
            float l0 = O0[8], l1 = lm[0];
            if (slow) {
                const float mn = fmaxf(m0, lm[1]);
                const float s0 = __builtin_amdgcn_exp2f(m0 - mn), s1 = __builtin_amdgcn_exp2f(lm[1] - mn);
                ka *= s0; kb *= s0; l0 *= s0; pa *= s1; pb *= s1; l1 *= s1;
            }
            const float inv = __builtin_amdgcn_rcpf(l0 + l1);
            ka = (ka + pa) * inv; kb = (kb + pb) * inv;
            auto s0 = __builtin_amdgcn_permlane32_swap(pack2(ka[0], ka[1]), pack2(kb[0], kb[1]), false, false);
            auto s1 = __builtin_amdgcn_permlane32_swap(pack2(ka[2], ka[3]), pack2(kb[2], kb[3]), false, false);
            o = u32x4{min(s0[0], POISON - 1), min(s1[0], POISON - 1), min(s0[1], POISON - 1), min(s1[1], POISON - 1)};
        }
        const int tokl = 8 * rg + 4 * kh + (r >> 3);
        st16_sc1(rsWS, WS_O + (tb * 32 + tokl) * 1024 + (8 * hg + (r & 7)) * 32 + h * 16, o);
        *(u32x4 LDSP*)(smw + K2_OI + tokl * 1024 + ((((r & 7) * 2 + h) ^ (tokl & 15)) << 4)) = o;
        const int tk = 4 * w + (lane >> 4), c = lane & 15;
        const unsigned poff = WS_O + (tb * 32 + tk) * 1024 + c * 16;
        const unsigned po0 = poff + ((hg + 1) & 3) * 256, po1 = poff + ((hg + 2) & 3) * 256, po2 = poff + ((hg + 3) & 3) * 256;
        wait_lgkm0();
        barrier();
        STAMP(5);
        __builtin_amdgcn_s_sleep(XSLEEP);
        u32x4 p0, p1, p2;
        bload16_sc1(p0, rsWS, po0); bload16_sc1(p1, rsWS, po1); bload16_sc1(p2, rsWS, po2);
        asm volatile("s_waitcnt vmcnt(0)" : "+v"(p0), "+v"(p1), "+v"(p2) :: "memory");
        for (unsigned spins = 0; spins < (1u << 20); ++spins) {
            const unsigned mx = max(max(max(max(p0[0], p0[1]), max(p0[2], p0[3])), max(max(p1[0], p1[1]), max(p1[2], p1[3]))),
                                    max(max(p2[0], p2[1]), max(p2[2], p2[3])));
            if (__all(mx != POISON)) break;
            bload16_sc1(p0, rsWS, po0); bload16_sc1(p1, rsWS, po1); bload16_sc1(p2, rsWS, po2);
            asm volatile("s_waitcnt vmcnt(0)" : "+v"(p0), "+v"(p1), "+v"(p2) :: "memory");
        }
        *(u32x4 LDSP*)(smw + K2_OI + tk * 1024 + (((16 + c) ^ (tk & 15)) << 4)) = p0;
        *(u32x4 LDSP*)(smw + K2_OI + tk * 1024 + (((32 + c) ^ (tk & 15)) << 4)) = p1;
        *(u32x4 LDSP*)(smw + K2_OI + tk * 1024 + (((48 + c) ^ (tk & 15)) << 4)) = p2;
        STAMP(6);
        wait_lgkm0();
        barrier();
        STAMP(7);
    } else {
        const int hw = w - 8;
        __builtin_amdgcn_s_setprio(3);
        const unsigned ko = hw < 2 ? hw * 32768 : K2_V + (hw - 2) * 34816;
        auto group = [&](int g) { dma4(rsWS, lane * 16, WS_KV + ko + g * 4096, lds0 + ko + g * 4096); };
        group(0);
        if (hw == 0 && lane < 16) *(unsigned LDSP*)(smw + K2_CNT + lane * 4) = 0;
        wait_lgkm0();
        barrier();
        group(1);
        wait_vm<4>(); lds_post(smw + K2_CNT + 4 * C_ST0, lane);
        group(2);
        wait_vm<4>(); lds_post(smw + K2_CNT + 4 * C_ST1, lane);
        group(3);
        wait_vm<4>(); lds_post(smw + K2_CNT + 4 * C_ST2, lane);
        group(4); group(5); group(6); group(7);
        if (hw >= 2) dma2(rsWS, lane * 16, WS_KV + ko + 32768, lds0 + ko + 32768);
        const int ch = hw >> 1, st = hw & 1;
        dma4(mk_rsrc(ssmk, DM * S_LEN * 2), st * 2 + lane * 16, (bid * 2 + ch) * 4096, lds0 + K2_H + (ch * 2 + st) * K2_HC + 32);
        dma2(rsWS, lane * 16, WS_UR + (bid * 2 + ch) * 4096 + st * 2048, lds0 + K2_U + ch * K2_UC + st * 2048);
        u32x4 q0, q1; u32x2 kn, vm;
        {
            const unsigned qoff = WS_Q + (unsigned)(tb * 32 + 8 * hw + (r >> 3)) * 1024 + (8 * hg + (r & 7)) * 32 + h * 16;
            bload16(q0, rsWS, qoff); bload16(q1, rsWS, qoff + 4096);
            bload8(kn, rsWS, WS_KN + lane * 8); bload8(vm, rsWS, WS_KN + 512 + lane * 8);
        }
        wait_vm<10>(); lds_post(smw + K2_CNT + 4 * C_ST3, lane);
        wait_vm<4>();  lds_post(smw + K2_CNT + 4 * (C_CH0 + ch), lane);
        asm volatile("s_waitcnt vmcnt(0)" : "+v"(q0), "+v"(q1), "+v"(kn), "+v"(vm) :: "memory");
        {
            const float kk = u2f(wave_umax(max(kn[0], kn[1]))), vx = u2f(wave_umax(max(vm[0], vm[1])));
            float qq0 = 0.f, qq1 = 0.f;
#pragma unroll
            for (int i = 0; i < 4; ++i) { qq0 = dot2(q0[i], q0[i], qq0); qq1 = dot2(q1[i], q1[i], qq1); }
            float qq = fmaxf(qq0 + __shfl_xor(qq0, 32), qq1 + __shfl_xor(qq1, 32));
            if (!(qq0 == qq0) || !(qq1 == qq1)) qq = __builtin_inff();
            const bool fast = __all(qq * kk <= 3600.f && vx <= 1.0e12f);
            if (lane == 0) *(unsigned LDSP*)(smw + K2_CNT + 4 * (C_SLOW + hw)) = fast ? 0u : 1u;
        }
        lds_wait(sm + K2_CNT + 4 * (C_CH0 + ch), 2);
        STAMP(1);
        {
            const u16 h0 = *(const u16 LDSP*)(sm + K2_H + (ch * 2) * K2_HC + 32);
            if (lane < 16) {
                *(u16 LDSP*)(smw + K2_H + (ch * 2) * K2_HC + lane * 2) = 0;
                *(u16 LDSP*)(smw + K2_H + (ch * 2 + 1) * K2_HC + lane * 2) = lane == 15 ? h0 : (u16)0;
            }
            if (lane < 32) *(u32x4 LDSP*)(smw + K2_U + ch * K2_UC + 4096 + lane * 16) = u32x4{0, 0, 0, 0};
            wait_lgkm0();
            const float dskip = bflo((unsigned)ssmD[bid * 2 + ch]);
            const unsigned yoff = WS_YT + (bid * 2 + ch) * 4096;
            if (st) conv_wave<1>(sm, ch, lane, dskip, rsWS, yoff); else conv_wave<0>(sm, ch, lane, dskip, rsWS, yoff);
        }
        STAMP(2);
        wait_vm<0>();
        if (lds_inc(smw + K2_CNT + 4 * C_YST, lane) == 3) {
            if (lane == 0) __hip_atomic_store((unsigned*)(ws + WS_FY) + bid, 1u, __ATOMIC_RELAXED, __HIP_MEMORY_SCOPE_AGENT);
        }
        lds_wait(sm + K2_CNT + 4 * C_YST, 4);
        const int nt = 4 * hg + hw;
        {
            const rsrc_t rsX = mk_rsrc(x, S_LEN * DM * 2);
            const unsigned vxo = (tb * 32 + (lane >> 2)) * 1024 + nt * 64 + (lane & 3) * 16;
            dma1(rsX, vxo, 0, lds0 + K2_XP + hw * 2048);
            dma1(rsX, vxo, 16 * 1024, lds0 + K2_XP + hw * 2048 + 1024);
        }
        const float g = ((const float*)(ws + WS_G))[tb * 32 + r];
        u32x4 wf[32];
#pragma unroll
        for (int i = 0; i < 32; ++i) wf[i] = *(const u32x4*)(ws + WS_WO + (nt * 32 + ((8 * hg + i) & 31)) * 1024 + lane * 16);
        {
            unsigned spins = 0;
            const unsigned* fp = (const unsigned*)(ws + WS_FY) + 16 * nt + (lane & 15);
            while (!__all(__hip_atomic_load(fp, __ATOMIC_RELAXED, __HIP_MEMORY_SCOPE_AGENT) != 0) && ++spins < (1u << 20)) __builtin_amdgcn_s_sleep(8);
            asm volatile("" ::: "memory");
#pragma unroll
            for (int i = 0; i < 2; ++i) {
                const u32x4 yv = ld16_sc1(rsWS, WS_YT + (nt * 32 + 16 * i + (lane >> 2)) * 4096 + tb * 64 + (lane & 3) * 16);
                *(u32x4 LDSP*)(smw + K2_YP + hw * 2048 + i * 1024 + lane * 16) = yv;
            }
        }
        wait_vm<0>();
        wait_lgkm0();
        u32x2 xy[4];
        {
            const unsigned G = lane >> 4, qq = (lane & 15) >> 2, pp = lane & 3;
#pragma unroll
            for (int q = 0; q < 4; ++q) {
                const u32x2 y = ldstr(sm + K2_YP + hw * 2048 + (8 * q + 4 * (G >> 1) + qq) * 64 + (16 * (G & 1) + 4 * pp) * 2);
                const u32x2 xx = *(const u32x2 LDSP*)(sm + K2_XP + hw * 2048 + r * 64 + (8 * q + 4 * h) * 2);
                xy[q] = u32x2{pack2(bflo(xx[0]) * bflo(y[0]) * g, bfhi(xx[0]) * bfhi(y[0]) * g), pack2(bflo(xx[1]) * bflo(y[1]) * g, bfhi(xx[1]) * bfhi(y[1]) * g)};
            }
        }
        float g1 = 1.f - g;
        asm volatile("" : "+v"(xy[0]), "+v"(xy[1]), "+v"(xy[2]), "+v"(xy[3]), "+v"(g1));
        STAMP(3);
        barrier();
        {
            const u32x4 slow4 = lds16(sm + K2_CNT + 4 * C_SLOW);
            if (__builtin_amdgcn_readfirstlane((int)(slow4[0] | slow4[1] | slow4[2] | slow4[3])) != 0) barrier();
        }
        barrier();
        barrier();
        f32x16 acc = {};
#pragma unroll
        for (int i = 0; i < 8; ++i) acc = mfma32(wf[i], lds16(sm + K2_OI + r * 1024 + (((2 * i + h) ^ (r & 15)) << 4)), acc);
        const unsigned ob = lds0 + K2_OI + r * 1024 + ((h ^ (r & 15)) << 4);
        STAMP(4);
        barrier();
        STAMP(5);
        oproj12<8>(acc, wf + 8, ob);
        oproj12<20>(acc, wf + 20, ob);
        STAMP(6);
        u32x2 p[4];
#pragma unroll
        for (int q = 0; q < 4; ++q) {
            p[q][0] = pack2(fmaf(g1, acc[4 * q], bflo(xy[q][0])), fmaf(g1, acc[4 * q + 1], bfhi(xy[q][0])));
            p[q][1] = pack2(fmaf(g1, acc[4 * q + 2], bflo(xy[q][1])), fmaf(g1, acc[4 * q + 3], bfhi(xy[q][1])));
        }
        u32x4 b0, b1;
        quads_to_rows(p, b0, b1);
        char* op = (char*)out + (size_t)(tb * 32 + r) * 1024 + nt * 64 + h * 16;
        *(u32x4*)op = b0;
        *(u32x4*)(op + 32) = b1;
        STAMP(7);
    }
    STAMP_FLUSH(1);
}

extern "C" void kernel_launch(void* const* d_in, const int* in_sizes, int n_in, void* d_out, int out_size, void* d_ws, size_t ws_size,
                              hipStream_t stream) {
    const u16* A_q = (const u16*)d_in[0];
    const u16* A_k = (const u16*)d_in[1];
    const u16* A_v = (const u16*)d_in[2];
    const u16* A_shift = (const u16*)d_in[3];
    const u16* A_ssmk = (const u16*)d_in[4];
    const u16* A_ssmD = (const u16*)d_in[5];
    const u16* wq = (const u16*)d_in[7];
    const u16* wk = (const u16*)d_in[8];
    const u16* wv = (const u16*)d_in[9];
    const u16* wo = (const u16*)d_in[10];
    const u16* gw = (const u16*)d_in[11];
    char* ws = (char*)d_ws;
    gm_k1_proj<<<256, 512, 0, stream>>>(A_q, A_k, A_v, A_shift, wq, wk, wv, wo, gw, A_ssmk, (const u16*)d_out, ws);
    gm_k2_main<<<256, 768, 0, stream>>>(A_q, A_ssmk, A_ssmD, ws, (u16*)d_out);
}
```
